```python
import torch

def _softmax(x, axis=-1):
    return torch.softmax(x, dim=axis)

def _gelu(x):
    return torch.nn.functional.gelu(x, approximate='tanh')

def _silu(x):
    return torch.nn.functional.silu(x)

def _sigmoid(x):
    return torch.sigmoid(x)

def _layernorm(x, axis=-1, eps=1e-05):
    m = x.mean(dim=axis, keepdim=True)
    v = x.var(dim=axis, keepdim=True, unbiased=False)
    return (x - m) / torch.sqrt(v + eps)

def _scatter_add(tgt, idx, src, axis=0):
    return tgt.clone().index_add_(axis, idx, src)

def _segment_sum(src, seg, n):
    out = torch.zeros((n,) + src.shape[1:], dtype=src.dtype)
    return out.index_add_(0, seg, src)

def _pad(x, pw):
    flat = tuple((v for pair in reversed(pw) for v in pair))
    return torch.nn.functional.pad(x, flat)
kernel_name = 'gated_mix__memory_aug_recurrent_mem_tr__softmax_full_mha__prompt'

def setup_inputs():

    _w = lambda a: (lambda t: t.to(torch.bfloat16) if t.is_floating_point() else t)(torch.as_tensor(a))

    def _A_setup():
        B, S, Dm = (1, 2048, 512)
        H, Dh = (32, 128)
        M, seg = (8, 32)
        seg = min(seg, S)
        S = S // seg * seg
        return {'x': _w((lambda *s: torch.randn(*s, dtype=torch.bfloat16) * 0.05)(B, S, Dm)), 'mem0': _w((lambda *s: torch.randn(*s, dtype=torch.bfloat16) * 0.8)(B, M, Dm)), 'Wq': _w((lambda *s: torch.randn(*s, dtype=torch.bfloat16) * 1.6)(Dm, H, Dh) * Dm ** (-0.5)), 'Wk': _w((lambda *s: torch.randn(*s, dtype=torch.bfloat16) * 1.6)(Dm, H, Dh) * Dm ** (-0.5)), 'Wv': _w((lambda *s: torch.randn(*s, dtype=torch.bfloat16) * 0.8)(Dm, H, Dh) * Dm ** (-0.5)), 'Wo': _w((lambda *s: torch.randn(*s, dtype=torch.bfloat16) * 0.8)(H, Dh, Dm) * (H * Dh) ** (-0.5)), 'seg_len': seg}
    _A_in = _A_setup()

    def _B_setup():
        B, S, D = (1, 2048, 512)
        return {'x': _w((lambda *s: torch.randn(*s, dtype=torch.bfloat16) * 0.05)(B, S, D)), 'wqkv': _w((lambda *s: torch.randn(*s, dtype=torch.bfloat16) * 0.8)(D, 3 * D)), 'wo': _w((lambda *s: torch.randn(*s, dtype=torch.bfloat16) * 0.05)(D, D))}
    _B_in = _B_setup()
    return {'A_x': _A_in['x'], 'A_mem0': _A_in['mem0'], 'A_Wq': _A_in['Wq'], 'A_Wk': _A_in['Wk'], 'A_Wv': _A_in['Wv'], 'A_Wo': _A_in['Wo'], 'A_seg_len': _A_in['seg_len'], 'B_x': _B_in['x'], 'B_wqkv': _B_in['wqkv'], 'B_wo': _B_in['wo'], 'gate_w': _w((lambda *s: torch.randn(*s, dtype=torch.bfloat16) * 0.05)(512) * 16.0)}

def reference(A_x, A_mem0, A_Wq, A_Wk, A_Wv, A_Wo, A_seg_len, B_x, B_wqkv, B_wo, gate_w):

    def _A_ref(x, mem0, Wq, Wk, Wv, Wo, seg_len):
        B, S, Dm = x.shape
        H, Dh = (Wq.shape[1], Wq.shape[2])
        M = mem0.shape[1]
        scale = Dh ** (-0.5)
        n_seg = S // seg_len
        L = M + seg_len
        idx = torch.arange(L)
        mask = (idx[:, None] < M) | (idx[:, None] >= idx[None, :])
        mem = mem0
        outs = []
        for s in range(n_seg):
            xs = x[:, s * seg_len:(s + 1) * seg_len, :].clone()
            h = torch.concatenate([mem, xs], axis=1)
            q = torch.einsum('btd,dhe->bhte', h, Wq)
            k = torch.einsum('btd,dhe->bhte', h, Wk)
            v = torch.einsum('btd,dhe->bhte', h, Wv)
            scores = torch.einsum('bhqe,bhke->bhqk', q, k) * scale
            scores = torch.where(mask[None, None], scores, -1000000000.0)
            p = _softmax(scores, axis=-1)
            o = torch.einsum('bhqk,bhke->bhqe', p, v)
            o = torch.einsum('bhte,hed->btd', o, Wo)
            mem = o[:, :M, :].clone()
            outs.append(o[:, M:, :])
        return torch.concatenate(outs, axis=1)

    def _B_ref(x, wqkv, wo):
        B, S, D = x.shape
        H = 32
        Dh = D // H
        qkv = (lambda x, *d: x.permute(*d))((x @ wqkv).reshape(B, S, 3, H, Dh), 2, 0, 3, 1, 4)
        q, k, v = (qkv[0], qkv[1], qkv[2])
        scale = Dh ** (-0.5)
        scores = torch.einsum('bhqd,bhkd->bhqk', q, k) * scale
        p = _softmax(scores, axis=-1)
        o = torch.einsum('bhqk,bhkd->bhqd', p, v)
        return (lambda x, *d: x.permute(*d))(o, 0, 2, 1, 3).reshape(B, S, D) @ wo
    x = A_x
    g = _sigmoid((x * gate_w).sum(-1, keepdims=True))
    a = _A_ref(x, A_mem0, A_Wq, A_Wk, A_Wv, A_Wo, A_seg_len)
    b = _B_ref(x, B_wqkv, B_wo)
    return g * a + (1 - g) * b
```

```cpp
#include <hip/hip_runtime.h>
#include <hip/hip_bf16.h>
#include <stdint.h>

typedef unsigned short u16;
typedef __attribute__((ext_vector_type(2))) float f32x2;
typedef __attribute__((ext_vector_type(4))) float f32x4;
typedef __attribute__((ext_vector_type(16))) float f32x16;
typedef __attribute__((ext_vector_type(2))) unsigned u32x2;
typedef __attribute__((ext_vector_type(4))) unsigned u32x4;
typedef __attribute__((ext_vector_type(8))) unsigned u32x8;
typedef __attribute__((ext_vector_type(2))) __bf16 bf16x2;
typedef __attribute__((ext_vector_type(4))) __bf16 bf16x4;
typedef __attribute__((ext_vector_type(8))) __bf16 bf16x8;
typedef __attribute__((ext_vector_type(16))) __bf16 bf16x16;
typedef __attribute__((ext_vector_type(4))) short s16x4;
#define LAS __attribute__((address_space(3)))
#define GAS __attribute__((address_space(1)))
#define DI __device__ __forceinline__
typedef LAS char* lptr;

constexpr int NSEG = 64;
constexpr int LDS_BYTES = 160 * 1024;
constexpr unsigned SPIN_CAP = 1u << 18;

constexpr int L_NEAR = 0, L_INIT = 1, L_TMO = 2, L_PXP = 8, L_HXP = 40, L_FOP = 104, L_BQC = 136, L_BAC = 148, L_BOC = 152, L_END = 184;
constexpr size_t CTRL_BYTES = (size_t)L_END * 256;
constexpr unsigned X_PART = 0, X_MEM = 512 * 1024;
constexpr unsigned KQV_K = 0, KQV_V = 9216, KQV_Q = 17408, KQV_SZ = 26624;
constexpr size_t O_KQV = 0;
constexpr size_t O_OX = O_KQV + (size_t)64 * 32 * KQV_SZ;
constexpr size_t O_FP = O_OX + (size_t)2048 * 4096 * 2;
constexpr size_t O_QB = O_FP + (size_t)64 * 32 * 16384;
constexpr size_t O_KB = O_QB + (size_t)32 * 65536;
constexpr size_t O_VB = O_KB + (size_t)32 * 65536;
constexpr size_t O_OB = O_VB + (size_t)32 * 65536;
constexpr size_t O_BP = O_OB + (size_t)32 * 65536;
constexpr size_t O_G = O_BP + (size_t)2048 * 512 * 4;
constexpr size_t BIG_BYTES = O_G + 2048 * 4;

struct Params {
    const u16* x; const u16* mem0; const u16* Wq; const u16* Wk; const u16* Wv; const u16* Wo;
    const u16* wqkv; const u16* wo; const u16* gw;
    u16* out; unsigned* ctrl; char* exch; char* big;
};

DI unsigned rfl(unsigned v) { return __builtin_amdgcn_readfirstlane(v); }
DI void bar() { asm volatile("s_waitcnt lgkmcnt(0)\n\ts_barrier" ::: "memory"); }
DI void vm0() { asm volatile("s_waitcnt vmcnt(0)" ::: "memory"); }
template <int N> DI void vmn() { asm volatile("s_waitcnt vmcnt(%0)" :: "n"(N) : "memory"); }
DI unsigned pk2(float a, float b) { f32x2 v = {a, b}; return __builtin_bit_cast(unsigned, __builtin_convertvector(v, bf16x2)); }
DI float bf_lo(unsigned u) { return __uint_as_float(u << 16); }
DI float bf_hi(unsigned u) { return __uint_as_float(u & 0xFFFF0000u); }
DI u32x4 lds_r16(lptr p) { return *(LAS const u32x4*)p; }
DI u32x2 lds_r8(lptr p) { return *(LAS const u32x2*)p; }
DI void lds_w16(lptr p, u32x4 v) { *(LAS u32x4*)p = v; }
DI void lds_w8(lptr p, u32x2 v) { *(LAS u32x2*)p = v; }
DI u32x2 lds_tr(lptr p) { return __builtin_bit_cast(u32x2, __builtin_amdgcn_ds_read_tr16_b64_v4bf16((LAS bf16x4*)p)); }
DI unsigned lds_addr(lptr p) { return (unsigned)(size_t)p; }
DI f32x4 mfma16(u32x4 a, u32x4 b, f32x4 c) { return __builtin_amdgcn_mfma_f32_16x16x32_bf16(__builtin_bit_cast(bf16x8, a), __builtin_bit_cast(bf16x8, b), c, 0, 0, 0); }
DI f32x16 mfma32(u32x4 a, u32x4 b, f32x16 c) { return __builtin_amdgcn_mfma_f32_32x32x16_bf16(__builtin_bit_cast(bf16x8, a), __builtin_bit_cast(bf16x8, b), c, 0, 0, 0); }
DI f32x4 smf(u32x4 a, u32x8 b, f32x4 c, int idx) { return __builtin_amdgcn_smfmac_f32_16x16x64_bf16(__builtin_bit_cast(bf16x8, a), __builtin_bit_cast(bf16x16, b), c, idx, 0, 0); }
DI u32x4 lo4(u32x8 w) { return __builtin_shufflevector(w, w, 0, 1, 2, 3); }
DI u32x4 hi4(u32x8 w) { return __builtin_shufflevector(w, w, 4, 5, 6, 7); }
DI float fold32(float a, float b) { auto r = __builtin_amdgcn_permlane32_swap(__float_as_uint(a), __float_as_uint(b), false, false); return __uint_as_float(r[0]) + __uint_as_float(r[1]); }
DI float fold16(float a, float b) { auto r = __builtin_amdgcn_permlane16_swap(__float_as_uint(a), __float_as_uint(b), false, false); return __uint_as_float(r[0]) + __uint_as_float(r[1]); }
DI float max32(float a) { auto r = __builtin_amdgcn_permlane32_swap(__float_as_uint(a), __float_as_uint(a), false, false); return fmaxf(__uint_as_float(r[0]), __uint_as_float(r[1])); }
DI float max16(float a) { auto r = __builtin_amdgcn_permlane16_swap(__float_as_uint(a), __float_as_uint(a), false, false); return fmaxf(__uint_as_float(r[0]), __uint_as_float(r[1])); }
DI float ex2(float x) { return __builtin_amdgcn_exp2f(x); }

typedef __amdgpu_buffer_rsrc_t rsrc_t;
DI rsrc_t mkrs(const void* p, unsigned bytes) { return __builtin_amdgcn_make_buffer_rsrc((void*)p, 0, bytes, 0x00020000); }
DI u32x4 ld16_dev(rsrc_t r, unsigned off) { return __builtin_amdgcn_raw_buffer_load_b128(r, off, 0, 16); }
DI void st16_dev(rsrc_t r, unsigned off, u32x4 v) { __builtin_amdgcn_raw_buffer_store_b128(v, r, off, 0, 16); }
DI void st8_dev(rsrc_t r, unsigned off, u32x2 v) { __builtin_amdgcn_raw_buffer_store_b64(v, r, off, 0, 16); }
DI void st4_dev(rsrc_t r, unsigned off, unsigned v) { __builtin_amdgcn_raw_buffer_store_b32(v, r, off, 0, 16); }
DI u32x4 ld16_dev(rsrc_t r, unsigned off, unsigned soff) { return __builtin_amdgcn_raw_buffer_load_b128(r, off, soff, 16); }
DI void st16_dev(rsrc_t r, unsigned off, unsigned soff, u32x4 v) { __builtin_amdgcn_raw_buffer_store_b128(v, r, off, soff, 16); }
DI void st8_dev(rsrc_t r, unsigned off, unsigned soff, u32x2 v) { __builtin_amdgcn_raw_buffer_store_b64(v, r, off, soff, 16); }
DI void st4_dev(rsrc_t r, unsigned off, unsigned soff, unsigned v) { __builtin_amdgcn_raw_buffer_store_b32(v, r, off, soff, 16); }
DI int opaque(int v) { asm volatile("" : "+v"(v)); return v; }
DI unsigned ldw_dev(const unsigned* p) { return __hip_atomic_load((GAS const unsigned*)p, __ATOMIC_RELAXED, __HIP_MEMORY_SCOPE_AGENT); }
DI void stw_dev(unsigned* p, unsigned v) { __hip_atomic_store((GAS unsigned*)p, v, __ATOMIC_RELAXED, __HIP_MEMORY_SCOPE_AGENT); }
DI void addw_dev(unsigned* p, unsigned v) { __hip_atomic_fetch_add((GAS unsigned*)p, v, __ATOMIC_RELAXED, __HIP_MEMORY_SCOPE_AGENT); }
DI void dma16(const void* gsrc, unsigned lds_dst) {
    unsigned keep;
    asm volatile("s_mov_b32 %0, m0\n\ts_mov_b32 m0, %2\n\ts_nop 0\n\tglobal_load_lds_dwordx4 %1, off\n\ts_mov_b32 m0, %0" : "=&s"(keep) : "v"(gsrc), "s"(lds_dst) : "memory");
}
DI void dma16_dev(const void* gsrc, unsigned lds_dst) {
    unsigned keep;
    asm volatile("s_mov_b32 %0, m0\n\ts_mov_b32 m0, %2\n\ts_nop 0\n\tglobal_load_lds_dwordx4 %1, off sc1\n\ts_mov_b32 m0, %0" : "=&s"(keep) : "v"(gsrc), "s"(lds_dst) : "memory");
}
DI bool spin_fail(unsigned& spins, unsigned* ctrl) {
    if (++spins > SPIN_CAP) { stw_dev(ctrl + L_TMO * 64, 1u); return true; }
    return (spins & 1023u) == 0u && rfl(ldw_dev(ctrl + L_TMO * 64)) != 0u;
}
DI unsigned wait_words(unsigned* ctrl, int line0, int n, unsigned want, int lane) {
    const unsigned* w = ctrl + (line0 + (lane < n ? lane : 0)) * 64;
    for (unsigned spins = 0;;) {
        const unsigned v = ldw_dev(w);
        if (__all(v >= want)) {
            unsigned m = v;
            for (int d = 1; d < n; d <<= 1) m = min(m, (unsigned)__shfl_xor((int)m, d));
            return rfl(m);
        }
        if (spin_fail(spins, ctrl)) return 0;
        __builtin_amdgcn_s_sleep(4);
    }
}
DI unsigned xphys(unsigned off, unsigned c) { return (off >> 13) * 16384u + c * 8192u + (off & 8191u); }

DI unsigned swz_f(unsigned row) { return (row & 1u) | ((((row >> 1) ^ (row >> 2)) & 1u) << 1) | (((row >> 3) & 1u) << 2); }
DI void stage_half(const u16* src, unsigned row_stride_b, unsigned buf, int w, int lane) {
#pragma unroll
    for (int n = 0; n < 8; ++n) {
        const unsigned i = w * 8 + n, row = 4 * i + (lane >> 4), ch = lane & 15;
        const unsigned sch = (((ch >> 1) ^ swz_f(row)) << 1) | (ch & 1);
        dma16((const char*)src + (size_t)row * row_stride_b + sch * 16, buf + i * 1024);
    }
}
template <bool SPARSE> DI unsigned frag_row(int u, int lane) {
    const unsigned gb = lane >> 4, q = (lane >> 2) & 3;
    return SPARSE ? 32 * (u >> 1) + 16 * (gb >> 1) + 8 * (q >> 1) + 4 * (gb & 1) + 2 * (u & 1) + (q & 1)
                  : 32 * (u >> 1) + 8 * gb + 4 * (u & 1) + q;
}
template <bool SPARSE> DI u32x8 read_frag(lptr buf, int row0, int pair, int lane) {
    u32x8 r;
#pragma unroll
    for (int u = 0; u < 4; ++u) {
        const unsigned row = row0 + frag_row<SPARSE>(u, lane);
        const u32x2 t = lds_tr(buf + row * 256 + ((pair ^ swz_f(row)) * 32) + (lane & 3) * 8);
        r[2 * u] = t.x; r[2 * u + 1] = t.y;
    }
    return r;
}
template <bool SPARSE> DI void load_slice(u32x8 (&W)[8], const u16* src, unsigned row_stride_b, lptr smem, unsigned bufA, unsigned bufB, int w, int lane) {
    const unsigned base = lds_addr(smem);
    stage_half(src, row_stride_b, base + bufA, w, lane);
    stage_half(src + (size_t)256 * (row_stride_b / 2), row_stride_b, base + bufB, w, lane);
    vmn<8>(); bar();
#pragma unroll
    for (int kk = 0; kk < 4; ++kk) W[kk] = read_frag<SPARSE>(smem + bufA, 64 * kk, w, lane);
    vm0(); bar();
#pragma unroll
    for (int kk = 0; kk < 4; ++kk) W[4 + kk] = read_frag<SPARSE>(smem + bufB, 64 * kk, w, lane);
    bar();
}
template <bool DEV> DI void stage_rows(const char* src, size_t row_stride_b, unsigned buf, int w, int lane) {
#pragma unroll
    for (int n = 0; n < 4; ++n) {
        const unsigned row = 4 * w + n, c = (lane & ~15) | ((lane ^ row) & 15);
        if (DEV) dma16_dev(src + row * row_stride_b + c * 16, buf + row * 1024);
        else dma16(src + row * row_stride_b + c * 16, buf + row * 1024);
    }
}
DI lptr afrag_ptr(lptr tile, int ks, int lane) { const int row = lane & 15, c = 4 * ks + (lane >> 4); return tile + row * 1024 + ((c & ~15) | ((c ^ row) & 15)) * 16; }
DI u32x4 wfrag(const u32x8 (&W)[8], int ks) { return (ks & 1) ? hi4(W[ks >> 1]) : lo4(W[ks >> 1]); }

constexpr int H_WO = 0;
constexpr int H_KX = 65536;
constexpr int H_KX_SZ = 22528;
constexpr int H_MEM = H_KX + 2 * H_KX_SZ;
constexpr int H_QT = H_MEM + 8192;
constexpr int H_KT = H_QT + 2048;
constexpr int H_VT = H_KT + 2048;
constexpr int H_OI = H_VT + 2048;
constexpr int H_RED = H_OI + 2304;
constexpr int H_END = H_RED + 2048;
static_assert(H_END <= LDS_BYTES, "");
constexpr float SCL_A = 0.08838834764831845f * 1.4426950408889634f;

template <bool XQ> DI void head_attn(lptr smem, int kx, int wv, int half, int lane, f32x4 (&o)[2]) {
    const int c16 = lane & 15, g = lane >> 4, tq = (lane >> 2) & 3, tp = lane & 1;
    f32x4 s0 = {0, 0, 0, 0}, s1 = {0, 0, 0, 0}, s2 = {0, 0, 0, 0};
#pragma unroll
    for (int ks = 0; ks < 4; ++ks) {
        const int e0 = 32 * ks + 8 * g + tq;
        u32x4 qf;
        if (XQ) qf = lds_r16(smem + kx + 17408 + c16 * 272 + g * 16 + ks * 64);
        else { const u32x2 a = lds_tr(smem + H_QT + e0 * 16 + tp * 8), b = lds_tr(smem + H_QT + (e0 + 4) * 16 + tp * 8); qf = (u32x4){a.x, a.y, b.x, b.y}; }
        const u32x2 ka = lds_tr(smem + H_KT + e0 * 16 + tp * 8), kb = lds_tr(smem + H_KT + (e0 + 4) * 16 + tp * 8);
        const u32x4 k1 = lds_r16(smem + kx + c16 * 272 + g * 16 + ks * 64);
        const u32x4 k2 = lds_r16(smem + kx + c16 * 272 + g * 16 + ks * 64 + 16 * 272);
        s0 = mfma16((u32x4){ka.x, ka.y, kb.x, kb.y}, qf, s0);
        s1 = mfma16(k1, qf, s1);
        s2 = mfma16(k2, qf, s2);
    }
    const float NEG = -__builtin_inff();
    const int qi = 16 * half + c16;
#pragma unroll
    for (int r = 0; r < 4; ++r) {
        if (g >= 2) s0[r] = NEG;
        if (XQ) { if (4 * g + r > qi) s1[r] = NEG; if (16 + 4 * g + r > qi) s2[r] = NEG; }
    }
    float m = fmaxf(fmaxf(fmaxf(s0[0], s0[1]), fmaxf(s0[2], s0[3])), fmaxf(fmaxf(fmaxf(s1[0], s1[1]), fmaxf(s1[2], s1[3])), fmaxf(fmaxf(s2[0], s2[1]), fmaxf(s2[2], s2[3]))));
    m = max32(max16(m));
    const float mc = m * SCL_A;
    float l = 0;
#pragma unroll
    for (int r = 0; r < 4; ++r) {
        s0[r] = ex2(fmaf(s0[r], SCL_A, -mc)); s1[r] = ex2(fmaf(s1[r], SCL_A, -mc)); s2[r] = ex2(fmaf(s2[r], SCL_A, -mc));
        l += s0[r] + s1[r] + s2[r];
    }
    const u32x2 p0 = {pk2(s0[0], s0[1]), pk2(s0[2], s0[3])};
    const u32x4 px = {pk2(s1[0], s1[1]), pk2(s1[2], s1[3]), pk2(s2[0], s2[1]), pk2(s2[2], s2[3])};
    l = fold16(l, l); l = fold32(l, l);
    const float rl = 1.0f / l;
#pragma unroll
    for (int t = 0; t < 2; ++t) {
        const int e = 32 * wv + 16 * t + c16;
        u32x2 vm = lds_r8(smem + H_VT + e * 16 + (g & 1) * 8);
        if (g >= 2) vm = (u32x2){0u, 0u};
        const u32x4 vx = lds_r16(smem + kx + 9216 + e * 64 + ((g ^ ((4 - ((e >> 2) & 3)) & 3)) * 16));
        f32x4 a = {0, 0, 0, 0};
        a = mfma16(vx, px, a);
        a = mfma16((u32x4){vm.x, vm.y, 0u, 0u}, (u32x4){p0.x, p0.y, 0u, 0u}, a);
        o[t] = a * rl;
    }
}

DI void head_role(const Params& p, int i, lptr smem, int tid_) {
    const int tid = tid_, lane = tid & 63, w = rfl(tid >> 6);
    const int h = (i >> 3) * 4 + (i & 3), half = (i >> 2) & 1;
    const unsigned lbase = lds_addr(smem);
    unsigned* ctrl = p.ctrl;
    const rsrc_t rx = mkrs(p.exch, 2u << 20);
    if (i == 0 && tid == 0) {
        unsigned best[2];
        for (int pg = 0; pg < 2; ++pg) {
            unsigned* q = (unsigned*)(p.exch + pg * 8192); unsigned b = ~0u;
            for (int k = 0; k < 2; ++k) {
                unsigned xx = k;
                asm volatile("global_store_dword %0, %1, off sc1\n\ts_waitcnt vmcnt(0)" :: "v"(q), "v"(xx) : "memory");
                const unsigned long long t0 = __builtin_amdgcn_s_memtime();
                asm volatile("global_load_dword %0, %1, off sc1\n\ts_waitcnt vmcnt(0)" : "=v"(xx) : "v"(q) : "memory");
                const unsigned t = (unsigned)(__builtin_amdgcn_s_memtime() - t0); b = t < b ? t : b;
            }
            best[pg] = b;
        }
        stw_dev(ctrl + L_NEAR * 64, best[0] <= best[1] ? 1u : 2u);
    }
    u32x8 Wq[8], Wk[8], Wv[8];
    load_slice<true>(Wq, p.Wq + h * 128, 8192, smem, 65536, 0, w, lane);
    load_slice<true>(Wk, p.Wk + h * 128, 8192, smem, 65536, 0, w, lane);
    load_slice<true>(Wv, p.Wv + h * 128, 8192, smem, 65536, 0, w, lane);
    {
        const u16* src = p.Wo + (size_t)h * 128 * 512 + half * 256;
#pragma unroll
        for (int n = 0; n < 8; ++n) {
            const unsigned ii = w * 8 + n, sub = ii >> 5, row = 4 * (ii & 31) + (lane >> 4), ch = lane & 15;
            const unsigned sch = (((ch >> 1) ^ swz_f(row)) << 1) | (ch & 1);
            dma16((const char*)src + (size_t)row * 1024 + sub * 256 + sch * 16, lbase + 65536 + ii * 1024);
        }
        vm0(); bar();
#pragma unroll
        for (int t = 0; t < 2; ++t)
#pragma unroll
            for (int ks = 0; ks < 2; ++ks) {
                const int col16 = 2 * w + t;
                const u32x8 f = read_frag<true>(smem + 65536 + (col16 >> 3) * 32768, 64 * ks, col16 & 7, lane);
                lptr d = smem + H_WO + ((w * 2 + t) * 2 + ks) * 2048 + lane * 16;
                lds_w16(d, lo4(f)); lds_w16(d + 1024, hi4(f));
            }
        bar();
    }
    const unsigned nearw = wait_words(ctrl, L_NEAR, 1, 1, lane);
    if (!nearw) return;
    const unsigned cn = nearw - 1;
    st16_dev(rx, xphys(X_PART + i * 8192 + tid * 16, cn), (u32x4){0u, 0u, 0u, 0u});
    if (tid < 504) st16_dev(rx, xphys(X_MEM + (1 + (tid >> 3)) * 8192 + i * 128 + (tid & 7) * 16, cn), (u32x4){~0u, ~0u, ~0u, ~0u});
    vm0(); bar();
    if (tid == 0) addw_dev(ctrl + L_INIT * 64, 1u);
    const char* kqv = p.big + O_KQV + (size_t)h * KQV_SZ;
    unsigned px_known = wait_words(ctrl, L_PXP + h, 1, 1, lane);
    if (!px_known) return;
    auto prefetch_kx = [&](int s, int ln) {
        const char* src = kqv + (size_t)s * 32 * KQV_SZ + ln * 16;
        const unsigned dst = lbase + H_KX + (s & 1) * H_KX_SZ;
        dma16_dev(src + w * 1024, dst + w * 1024);
        dma16_dev(src + 8192 + w * 1024, dst + 8192 + w * 1024);
        if (w == 0) dma16_dev(src + 16384, dst + 16384);
        else if (w < 6) dma16_dev(src + KQV_Q + half * 4352 + (w - 1) * 1024, dst + 17408 + (w - 1) * 1024);
    };
    prefetch_kx(0, lane);
    if (!wait_words(ctrl, L_INIT, 1, 64, lane)) return;
    const int sidx = (lane & 8) ? 0xEEEE : 0x4444;
    const rsrc_t rox = mkrs(p.big + O_OX, 2048u * 8192u);

    for (int s = 0; s < NSEG; ++s) {
        const int kx = H_KX + (s & 1) * H_KX_SZ;
        const int tid = opaque(tid_), lane = tid & 63;
        u32x4 mv;
        if (s == 0) mv = *(const GAS u32x4*)(p.mem0 + (tid & 7) * 512 + (tid >> 3) * 8);
        else {
            const unsigned soff = xphys(X_MEM + s * 8192, cn);
            for (unsigned spins = 0;;) {
                asm volatile("" ::: "memory");
                mv = ld16_dev(rx, tid * 16, soff);
                if (__all((mv.x != ~0u) & (mv.y != ~0u) & (mv.z != ~0u) & (mv.w != ~0u))) break;
                if (spin_fail(spins, ctrl)) return;
            }
        }
        lds_w16(smem + H_MEM + tid * 16, mv);
        vm0();
        bar();
        if (s + 1 < NSEG) {
            if (px_known < (unsigned)s + 2) { px_known = wait_words(ctrl, L_PXP + h, 1, s + 2, lane); if (!px_known) return; }
            prefetch_kx(s + 1, opaque(lane));
        }
        f32x4 aq = {0, 0, 0, 0}, ak = {0, 0, 0, 0}, av = {0, 0, 0, 0};
#pragma unroll
        for (int ks = 0; ks < 8; ++ks) {
            const u32x4 a = lds_r16(smem + H_MEM + ks * 1024 + lane * 16);
            aq = smf(a, Wq[ks], aq, sidx);
            ak = smf(a, Wk[ks], ak, sidx);
        }
        {
            const float f0 = fold32(aq[0], ak[0]), f1 = fold32(aq[1], ak[1]), f2 = fold32(aq[2], ak[2]), f3 = fold32(aq[3], ak[3]);
            lds_w8(smem + (lane < 32 ? H_QT : H_KT) + (16 * w + (lane & 15)) * 16 + ((lane >> 4) & 1) * 8, (u32x2){pk2(f0, f1), pk2(f2, f3)});
        }
#pragma unroll
        for (int ks = 0; ks < 8; ++ks) {
            const u32x4 a = lds_r16(smem + H_MEM + ks * 1024 + lane * 16);
            av = smf(a, Wv[ks], av, sidx);
        }
        {
            const float f0 = fold32(av[0], av[0]), f1 = fold32(av[1], av[1]), f2 = fold32(av[2], av[2]), f3 = fold32(av[3], av[3]);
            if (lane < 32) lds_w8(smem + H_VT + (16 * w + (lane & 15)) * 16 + ((lane >> 4) & 1) * 8, (u32x2){pk2(f0, f1), pk2(f2, f3)});
        }
        bar();
        f32x4 o[2];
        if (w < 4) {
            if (s + 1 < NSEG) {
                head_attn<false>(smem, kx, w, half, lane, o);
                const int t = (lane >> 3) & 1;
                const f32x4 ov = t ? o[1] : o[0];
                lds_w8(smem + H_OI + (lane & 7) * 272 + (32 * w + 16 * t + 4 * (lane >> 4)) * 2, (u32x2){pk2(ov[0], ov[1]), pk2(ov[2], ov[3])});
            }
        } else {
            head_attn<true>(smem, kx, w - 4, half, lane, o);
            const unsigned soff = (32 * s + 16 * half) * 8192 + (h * 128 + 32 * (w - 4)) * 2;
#pragma unroll
            for (int t = 0; t < 2; ++t)
                st8_dev(rox, (lane & 15) * 8192 + (16 * t + 4 * (lane >> 4)) * 2, soff, (u32x2){pk2(o[t][0], o[t][1]), pk2(o[t][2], o[t][3])});
        }
        if (s + 1 == NSEG) break;
        bar();
        const unsigned tag = (s & 1) ^ 1;
        {
            f32x4 a0 = {0, 0, 0, 0}, a1 = {0, 0, 0, 0};
#pragma unroll
            for (int ks = 0; ks < 2; ++ks) {
                const u32x4 a = lds_r16(smem + H_OI + (lane & 7) * 272 + (64 * ks + 16 * (lane >> 4) + 8 * ((lane >> 3) & 1)) * 2);
                lptr f0 = smem + H_WO + ((w * 2 + 0) * 2 + ks) * 2048 + lane * 16, f1 = smem + H_WO + ((w * 2 + 1) * 2 + ks) * 2048 + lane * 16;
                const u32x4 x0 = lds_r16(f0), x1 = lds_r16(f0 + 1024), y0 = lds_r16(f1), y1 = lds_r16(f1 + 1024);
                a0 = smf(a, __builtin_shufflevector(x0, x1, 0, 1, 2, 3, 4, 5, 6, 7), a0, sidx);
                a1 = smf(a, __builtin_shufflevector(y0, y1, 0, 1, 2, 3, 4, 5, 6, 7), a1, sidx);
            }
            u32x4 pv;
#pragma unroll
            for (int r = 0; r < 4; ++r) pv[r] = (__float_as_uint(fold32(a0[r], a1[r])) & ~1u) | tag;
            const int c = lane & 15, t = lane >> 5, rg = (lane >> 4) & 1;
            st16_dev(rx, (2 * t + (c >> 3)) * 16384 + ((c & 7) * 2 + rg) * 16, xphys(X_PART + (32 * half + 4 * w) * 8192 + h * 256, cn), pv);
        }
        {
            const unsigned soff = xphys(X_PART + i * 8192, cn);
            u32x4 v;
            for (unsigned spins = 0;;) {
                asm volatile("" ::: "memory");
                v = ld16_dev(rx, tid * 16, soff);
                if (__all((((v.x ^ tag) | (v.y ^ tag) | (v.z ^ tag) | (v.w ^ tag)) & 1u) == 0u)) break;
                if (spin_fail(spins, ctrl)) return;
            }
            const float a = fold32(__uint_as_float(v.x), __uint_as_float(v.z)), b = fold32(__uint_as_float(v.y), __uint_as_float(v.w));
            const float c = fold16(a, b);
            *(LAS float*)(smem + H_RED + w * 256 + ((4 * (lane & 1) + (lane >> 4)) * 8 + ((lane & 15) >> 1)) * 4) = c;
            vm0();
            bar();
            if (w == 0 && lane < 32) {
                f32x2 t = {0, 0};
#pragma unroll
                for (int ww = 0; ww < 8; ++ww) t += *(LAS const f32x2*)(smem + H_RED + ww * 256 + lane * 8);
                unsigned o2 = pk2(t.x, t.y);
                if (o2 == ~0u) o2 = 0x7FFF7FFFu;
                st4_dev(rx, lane * 4, xphys(X_MEM + (s + 1) * 8192 + i * 128, cn), o2);
            }
            if (tid == 64) stw_dev(ctrl + L_HXP * 64 + i * 64, s + 1);
        }
    }
    vm0(); bar();
    if (tid == 0) stw_dev(ctrl + L_HXP * 64 + i * 64, NSEG);
}

constexpr int P_X = 0;
constexpr int P_OUT = 65536;
DI void px_role(const Params& p, int h, lptr smem, int tid) {
    const int lane = tid & 63, w = rfl(tid >> 6);
    const unsigned lbase = lds_addr(smem);
    u32x8 Wq[8], Wk[8], Wv[8];
    load_slice<false>(Wk, p.Wk + h * 128, 8192, smem, 65536, 0, w, lane);
    load_slice<false>(Wv, p.Wv + h * 128, 8192, smem, 65536, 0, w, lane);
    load_slice<false>(Wq, p.Wq + h * 128, 8192, smem, 65536, 0, w, lane);
    const rsrc_t rk = mkrs(p.big + O_KQV, 64u * 32u * KQV_SZ);
    stage_rows<false>((const char*)p.x, 1024, lbase + P_X, w, lane);
    const int c16 = lane & 15, g = lane >> 4;
    for (int s = 0; s < NSEG; ++s) {
        vm0(); bar();
        if (s > 0 && tid == 0) stw_dev(p.ctrl + (L_PXP + h) * 64, s);
        if (s + 1 < NSEG) stage_rows<false>((const char*)p.x + (size_t)(s + 1) * 32768, 1024, lbase + P_X + ((s + 1) & 1) * 32768, w, lane);
        lptr tile = smem + P_X + (s & 1) * 32768;
        f32x4 q0 = {0, 0, 0, 0}, q1 = q0, k0 = q0, k1 = q0, v0 = q0, v1 = q0;
#pragma unroll
        for (int ks = 0; ks < 16; ++ks) {
            lptr a = afrag_ptr(tile, ks, lane);
            const u32x4 x0 = lds_r16(a), x1 = lds_r16(a + 16384);
            k0 = mfma16(wfrag(Wk, ks), x0, k0); k1 = mfma16(wfrag(Wk, ks), x1, k1);
            v0 = mfma16(x0, wfrag(Wv, ks), v0); v1 = mfma16(x1, wfrag(Wv, ks), v1);
            q0 = mfma16(wfrag(Wq, ks), x0, q0); q1 = mfma16(wfrag(Wq, ks), x1, q1);
        }
        lptr kc = smem + P_OUT + c16 * 272 + (16 * w + 4 * g) * 2;
        lds_w8(kc + KQV_K, (u32x2){pk2(k0[0], k0[1]), pk2(k0[2], k0[3])});
        lds_w8(kc + KQV_K + 16 * 272, (u32x2){pk2(k1[0], k1[1]), pk2(k1[2], k1[3])});
        lds_w8(kc + KQV_Q, (u32x2){pk2(q0[0], q0[1]), pk2(q0[2], q0[3])});
        lds_w8(kc + KQV_Q + 16 * 272, (u32x2){pk2(q1[0], q1[1]), pk2(q1[2], q1[3])});
        const int e = 16 * w + c16;
        lds_w16(smem + P_OUT + KQV_V + e * 64 + ((g ^ ((4 - ((e >> 2) & 3)) & 3)) * 16), (u32x4){pk2(v0[0], v0[1]), pk2(v0[2], v0[3]), pk2(v1[0], v1[1]), pk2(v1[2], v1[3])});
        bar();
#pragma unroll
        for (int n = 0; n < 4; ++n)
            if (n < 3 || tid < (KQV_SZ - 24576) / 16) st16_dev(rk, n * 8192 + tid * 16, (unsigned)(s * 32 + h) * KQV_SZ, lds_r16(smem + P_OUT + n * 8192 + tid * 16));
    }
    vm0(); bar();
    if (tid == 0) stw_dev(p.ctrl + (L_PXP + h) * 64, NSEG);
}

DI void seg_gemm(const u32x8 (&W)[8], lptr tile, int lane, f32x4& a0, f32x4& a1) {
    a0 = (f32x4){0, 0, 0, 0}; a1 = a0;
#pragma unroll
    for (int ks = 0; ks < 16; ++ks) {
        lptr a = afrag_ptr(tile, ks, lane);
        a0 = mfma16(wfrag(W, ks), lds_r16(a), a0);
        a1 = mfma16(wfrag(W, ks), lds_r16(a + 16384), a1);
    }
}
DI void put_f32_tile(lptr t, int w, int lane, f32x4 a0, f32x4 a1) {
    const int c16 = lane & 15, ch = 4 * w + (lane >> 4);
    lds_w16(t + c16 * 512 + (ch ^ c16) * 16, __builtin_bit_cast(u32x4, a0));
    lds_w16(t + (16 + c16) * 512 + (ch ^ c16) * 16, __builtin_bit_cast(u32x4, a1));
}
DI u32x4 get_f32_chunk(lptr t, int row, int ch) { return lds_r16(t + row * 512 + (ch ^ (row & 15)) * 16); }

constexpr int F_A = 0;
constexpr int F_T = 32768;
constexpr int F_R = 49152;
DI void fo_role(const Params& p, int j, lptr smem, int tid) {
    const int lane = tid & 63, w = rfl(tid >> 6);
    const unsigned lbase = lds_addr(smem);
    const int cg = j & 3, kg = j >> 2;
    u32x8 W[8];
    load_slice<false>(W, p.Wo + (size_t)kg * 512 * 512 + cg * 128, 1024, smem, 65536, 0, w, lane);
    const rsrc_t rf = mkrs(p.big + O_FP, 64u * 32u * 16384u);
    const rsrc_t rb = mkrs(p.big + O_BP, 2048u * 2048u + 8192u);
    int s1 = 0, s2 = 0;
    unsigned hx_known = 0, fo_known = 0, spins = 0;
    LAS unsigned* cmd = (LAS unsigned*)(smem + 60000 - 60000 % 16);
    while (s2 < NSEG) {
        if (w == 0) {
            unsigned act = 0;
            if (s1 < NSEG) {
                if (hx_known <= (unsigned)s1) {
                    unsigned v = ldw_dev(p.ctrl + (L_HXP + 8 * kg + (lane & 7)) * 64);
                    v = min(v, (unsigned)__shfl_xor((int)v, 1)); v = min(v, (unsigned)__shfl_xor((int)v, 2)); v = min(v, (unsigned)__shfl_xor((int)v, 4));
                    hx_known = rfl(v);
                }
                if (hx_known > (unsigned)s1) act = 1;
            }
            if (!act && s2 < s1) {
                if (fo_known <= (unsigned)s2) {
                    unsigned v = ldw_dev(p.ctrl + (L_FOP + (lane & 31)) * 64);
#pragma unroll
                    for (int d = 1; d < 32; d <<= 1) v = min(v, (unsigned)__shfl_xor((int)v, d));
                    fo_known = rfl(v);
                }
                if (fo_known > (unsigned)s2 && rfl(ldw_dev(p.ctrl + (L_BOC + (s2 >> 1)) * 64)) == 4u) act = 2;
            }
            if (!act) { if (spin_fail(spins, p.ctrl)) act = 3; else __builtin_amdgcn_s_sleep(8); } else spins = 0;
            if (lane == 0) *cmd = act;
        }
        bar();
        const unsigned act = rfl(*cmd);
        bar();
        if (act == 3) return;
        if (act == 1) {
            stage_rows<true>(p.big + O_OX + (size_t)s1 * 32 * 8192 + kg * 1024, 8192, lbase + F_A, w, lane);
            vm0(); bar();
            f32x4 a0, a1;
            seg_gemm(W, smem + F_A, lane, a0, a1);
            put_f32_tile(smem + F_T, w, lane, a0, a1);
            bar();
            const unsigned dst = ((unsigned)(s1 * 8 + kg) * 4 + cg) * 16384u;
#pragma unroll
            for (int n = 0; n < 2; ++n) { const int ci = n * 512 + tid; st16_dev(rf, dst + ci * 16, get_f32_chunk(smem + F_T, ci >> 5, ci & 31)); }
            vm0(); bar();
            ++s1;
            if (tid == 0) stw_dev(p.ctrl + (L_FOP + j) * 64, s1);
        } else if (act == 2) {
            const int row = s2 * 32 + j;
            const unsigned src = ((unsigned)(s2 * 8 + (tid >> 7)) * 4 + ((tid >> 5) & 3)) * 16384u + j * 512 + (tid & 31) * 16;
            const u32x4 v0 = ld16_dev(rf, src), v1 = ld16_dev(rf, src + 4 * 4 * 16384u);
            f32x2 bp = {0, 0}; float gt = 0;
            if (tid < 256) {
                const u32x2 b = __builtin_amdgcn_raw_buffer_load_b64(rb, row * 2048 + tid * 8, 0, 16);
                bp = __builtin_bit_cast(f32x2, b);
                gt = __uint_as_float(__builtin_amdgcn_raw_buffer_load_b32(rb, 2048u * 2048u + row * 4, 0, 16));
            }
            lds_w16(smem + F_R + (tid >> 7) * 2048 + (tid & 127) * 16, __builtin_bit_cast(u32x4, __builtin_bit_cast(f32x4, v0) + __builtin_bit_cast(f32x4, v1)));
            bar();
            if (tid < 256) {
                f32x2 t = {0, 0};
#pragma unroll
                for (int k = 0; k < 4; ++k) t += *(LAS const f32x2*)(smem + F_R + k * 2048 + tid * 8);
                *(GAS unsigned*)(p.out + (size_t)row * 512 + tid * 2) = pk2(fmaf(gt, t.x, bp.x), fmaf(gt, t.y, bp.y));
            }
            ++s2;
        }
    }
}

constexpr float SCL_B = 0.25f * 1.4426950408889634f;
constexpr int B_X = 0;
constexpr int B_T = 65536;
constexpr int B_G = 81920;
constexpr int B_K = 0, B_V = 65536, B_C = 131072;
template <bool SAFE> DI void b_attn_loop(lptr smem, int lane, u32x4 q0, u32x4 q1, f32x16& o0, f32x16& o1) {
    const int e = lane & 31;
    lptr kp = smem + B_K + lane * 16;
    lptr vp = e < 16 ? smem + B_V + (lane >> 5) * 256 + e * 16 : smem + B_C + (e == 16 ? 0 : 16);
    const int vstep = e < 16 ? 1024 : 0, vs = e < 16 ? 512 : 0;
    f32x16 z;
#pragma unroll
    for (int r = 0; r < 16; ++r) z[r] = 0;
    o0 = z; o1 = z;
    float m0 = -__builtin_inff(), m1 = m0;
#pragma unroll 2
    for (int kt = 0; kt < 64; ++kt) {
        const u32x4 kf = lds_r16(kp + kt * 1024);
        const u32x4 va = lds_r16(vp), vb = lds_r16(vp + vs);
        vp += vstep;
        f32x16 s0 = mfma32(kf, q0, z), s1 = mfma32(kf, q1, z);
        if (!SAFE) {
            float t0 = s0[0], t1 = s1[0];
#pragma unroll
            for (int r = 1; r < 16; ++r) { t0 = fmaxf(t0, s0[r]); t1 = fmaxf(t1, s1[r]); }
            t0 = fmaxf(max32(t0), m0); t1 = fmaxf(max32(t1), m1);
            const float c0 = ex2(m0 - t0), c1 = ex2(m1 - t1);
            m0 = t0; m1 = t1;
#pragma unroll
            for (int r = 0; r < 16; ++r) { s0[r] -= t0; s1[r] -= t1; o0[r] *= c0; o1[r] *= c1; }
        }
        u32x4 pa, pb, pc, pd;
#pragma unroll
        for (int r = 0; r < 4; ++r) {
            pa[r] = pk2(ex2(s0[2 * r]), ex2(s0[2 * r + 1])); pb[r] = pk2(ex2(s0[8 + 2 * r]), ex2(s0[9 + 2 * r]));
            pc[r] = pk2(ex2(s1[2 * r]), ex2(s1[2 * r + 1])); pd[r] = pk2(ex2(s1[8 + 2 * r]), ex2(s1[9 + 2 * r]));
        }
        o0 = mfma32(va, pa, o0); o0 = mfma32(vb, pb, o0);
        o1 = mfma32(va, pc, o1); o1 = mfma32(vb, pd, o1);
    }
}
DI float sq8(u32x4 v) {
    float s = 0;
#pragma unroll
    for (int r = 0; r < 4; ++r) { const float a = bf_lo(v[r]), b = bf_hi(v[r]); s = fmaf(a, a, s); s = fmaf(b, b, s); }
    return s;
}
DI void b_role(const Params& p, int i, lptr smem, int tid) {
    const int lane = tid & 63, w = rfl(tid >> 6), c16 = lane & 15, g = lane >> 4;
    const unsigned lbase = lds_addr(smem);
    u32x8 W[8];
    if (i < 96) {
        const int cg = i % 12, rr = i / 12, kind = cg >> 2, head = 8 * (cg & 3) + w;
        load_slice<false>(W, p.wqkv + cg * 128, 3072, smem, 65536, 0, w, lane);
        const rsrc_t ro = mkrs(p.big + O_QB + (size_t)kind * 32 * 65536, 32u * 65536u);
        stage_rows<false>((const char*)p.x + (size_t)rr * 8 * 32768, 1024, lbase + B_X, w, lane);
        for (int n = 0; n < 8; ++n) {
            vm0(); bar();
            if (n + 1 < 8) stage_rows<false>((const char*)p.x + (size_t)(rr * 8 + n + 1) * 32768, 1024, lbase + B_X + ((n + 1) & 1) * 32768, w, lane);
            lptr tile = smem + B_X + (n & 1) * 32768;
            const unsigned tb = head * 65536u + (rr * 8 + n) * 1024u;
            if (kind < 2) {
                f32x4 a0, a1;
                seg_gemm(W, tile, lane, a0, a1);
                if (kind == 0) { a0 *= SCL_B; a1 *= SCL_B; }
                const unsigned o = tb + (g >> 1) * 512 + c16 * 16 + (g & 1) * 8;
                st8_dev(ro, o, (u32x2){pk2(a0[0], a0[1]), pk2(a0[2], a0[3])});
                st8_dev(ro, o + 256, (u32x2){pk2(a1[0], a1[1]), pk2(a1[2], a1[3])});
            } else {
                f32x4 a0 = {0, 0, 0, 0}, a1 = a0;
#pragma unroll
                for (int ks = 0; ks < 16; ++ks) {
                    lptr a = afrag_ptr(tile, ks, lane);
                    a0 = mfma16(lds_r16(a), wfrag(W, ks), a0);
                    a1 = mfma16(lds_r16(a + 16384), wfrag(W, ks), a1);
                }
                const unsigned o = tb + (g & 1) * 256 + c16 * 16 + (g >> 1) * 8;
                st8_dev(ro, o, (u32x2){pk2(a0[0], a0[1]), pk2(a0[2], a0[3])});
                st8_dev(ro, o + 512, (u32x2){pk2(a1[0], a1[1]), pk2(a1[2], a1[3])});
            }
        }
        vm0(); bar();
        if (tid == 0) addw_dev(p.ctrl + (L_BQC + cg) * 64, 1u);
    }
    {
        const int h = i >> 2, qq = i & 3;
        if (w == 0) {
            const unsigned* cw = p.ctrl + (L_BQC + (h >> 3) + 4 * (lane < 3 ? lane : 0)) * 64;
            unsigned ok = 1;
            for (unsigned spins = 0;;) { if (__all(ldw_dev(cw) >= 8u)) break; if (spin_fail(spins, p.ctrl)) { ok = 0; break; } __builtin_amdgcn_s_sleep(8); }
            if (lane == 0) *(LAS unsigned*)(smem + B_C + 64) = ok;
        }
        bar();
        if (!rfl(*(LAS unsigned*)(smem + B_C + 64))) return;
        const char* kb = p.big + O_KB + (size_t)h * 65536, *vb = p.big + O_VB + (size_t)h * 65536;
#pragma unroll
        for (int n = 0; n < 8; ++n) {
            dma16_dev(kb + (w * 8 + n) * 1024 + lane * 16, lbase + B_K + (w * 8 + n) * 1024);
            dma16_dev(vb + (w * 8 + n) * 1024 + lane * 16, lbase + B_V + (w * 8 + n) * 1024);
        }
        const rsrc_t rq = mkrs(p.big + O_QB + (size_t)h * 65536, 65536u);
        const u32x4 q0 = ld16_dev(rq, (qq * 16 + 2 * w) * 1024 + lane * 16), q1 = ld16_dev(rq, (qq * 16 + 2 * w + 1) * 1024 + lane * 16);
        if (tid < 8) *(LAS unsigned*)(smem + B_C + tid * 4) = tid < 4 ? 0x3F803F80u : 0u;
        vm0(); bar();
        float kn = 0;
#pragma unroll
        for (int n = 0; n < 8; ++n) { const float t = sq8(lds_r16(smem + B_K + (w * 8 + n) * 1024 + lane * 16)); kn = fmaxf(kn, t + __shfl_xor(t, 32)); }
#pragma unroll
        for (int d = 1; d < 32; d <<= 1) kn = fmaxf(kn, __shfl_xor(kn, d));
        if (lane == 0) *(LAS float*)(smem + B_C + 128 + w * 4) = kn;
        bar();
#pragma unroll
        for (int n = 0; n < 8; ++n) kn = fmaxf(kn, *(LAS const float*)(smem + B_C + 128 + n * 4));
        float qn; { const float t0 = sq8(q0), t1 = sq8(q1); qn = fmaxf(t0 + __shfl_xor(t0, 32), t1 + __shfl_xor(t1, 32)); }
#pragma unroll
        for (int d = 1; d < 32; d <<= 1) qn = fmaxf(qn, __shfl_xor(qn, d));
        const bool safe = rfl(__float_as_uint(qn * kn)) <= __float_as_uint(60.0f * 60.0f);
        f32x16 o0, o1;
        if (safe) b_attn_loop<true>(smem, lane, q0, q1, o0, o1); else b_attn_loop<false>(smem, lane, q0, q1, o0, o1);
        const rsrc_t ro = mkrs(p.big + O_OB + (size_t)h * 65536, 65536u);
#pragma unroll
        for (int t = 0; t < 2; ++t) {
            const f32x16& o = t ? o1 : o0;
            auto lr = __builtin_amdgcn_permlane32_swap(__float_as_uint(o[8]), __float_as_uint(o[8]), false, false);
            const float rl = 1.0f / __uint_as_float(lr[0]);
            const unsigned a = pk2(o[0] * rl, o[1] * rl), b = pk2(o[2] * rl, o[3] * rl), c = pk2(o[4] * rl, o[5] * rl), d = pk2(o[6] * rl, o[7] * rl);
            auto x = __builtin_amdgcn_permlane32_swap(a, c, false, false);
            auto y = __builtin_amdgcn_permlane32_swap(b, d, false, false);
            st16_dev(ro, (qq * 512 + w * 64 + t * 32 + (lane & 31)) * 32 + (lane >> 5) * 16, (u32x4){x[0], y[0], x[1], y[1]});
        }
        vm0(); bar();
        if (tid == 0) addw_dev(p.ctrl + (L_BAC + qq) * 64, 1u);
    }
    {
        const int cg = i & 3, rr = i >> 2;
        load_slice<false>(W, p.wo + cg * 128, 1024, smem, 65536, 0, w, lane);
        {
            const u32x4 gw = *(const GAS u32x4*)(p.gw + lane * 8);
#pragma unroll
            for (int n = 0; n < 8; ++n) {
                const u32x4 xv = *(const GAS u32x4*)(p.x + (size_t)(64 * rr + 8 * w + n) * 512 + lane * 8);
                float z = 0;
#pragma unroll
                for (int r = 0; r < 4; ++r) { z = fmaf(bf_lo(xv[r]), bf_lo(gw[r]), z); z = fmaf(bf_hi(xv[r]), bf_hi(gw[r]), z); }
#pragma unroll
                for (int d = 1; d < 64; d <<= 1) z += __shfl_xor(z, d);
                if (lane == 0) *(LAS float*)(smem + B_G + (8 * w + n) * 4) = 1.0f / (1.0f + __expf(-z));
            }
        }
        if (w == 0) {
            unsigned ok = 1;
            for (unsigned spins = 0;;) { if (rfl(ldw_dev(p.ctrl + (L_BAC + (rr >> 3)) * 64)) >= 32u) break; if (spin_fail(spins, p.ctrl)) { ok = 0; break; } __builtin_amdgcn_s_sleep(8); }
            if (lane == 0) *(LAS unsigned*)(smem + B_G + 512) = ok;
        }
        bar();
        if (!rfl(*(LAS unsigned*)(smem + B_G + 512))) return;
        const rsrc_t rb = mkrs(p.big + O_BP, 2048u * 2048u + 8192u);
        const char* ob = p.big + O_OB;
        for (int n = 0; n < 2; ++n) {
            const int row0 = 64 * rr + 32 * n;
#pragma unroll
            for (int k = 0; k < 4; ++k) {
                const unsigned row = 4 * w + k, c = (lane & ~15) | ((lane ^ row) & 15);
                dma16_dev(ob + (size_t)(c >> 1) * 65536 + (row0 + row) * 32 + (c & 1) * 16, lbase + B_X + row * 1024);
            }
            vm0(); bar();
            f32x4 a0, a1;
            seg_gemm(W, smem + B_X, lane, a0, a1);
            const float g0 = 1.0f - *(LAS const float*)(smem + B_G + (32 * n + c16) * 4), g1 = 1.0f - *(LAS const float*)(smem + B_G + (32 * n + 16 + c16) * 4);
            put_f32_tile(smem + B_T, w, lane, a0 * g0, a1 * g1);
            bar();
#pragma unroll
            for (int k = 0; k < 2; ++k) { const int ci = k * 512 + tid; st16_dev(rb, (row0 + (ci >> 5)) * 2048 + cg * 512 + (ci & 31) * 16, get_f32_chunk(smem + B_T, ci >> 5, ci & 31)); }
            bar();
        }
        if (cg == 0 && tid < 64) st4_dev(rb, 2048u * 2048u + (64 * rr + tid) * 4, *(LAS const unsigned*)(smem + B_G + tid * 4));
        vm0(); bar();
        if (tid == 0) addw_dev(p.ctrl + (L_BOC + rr) * 64, 1u);
    }
}

__global__ void __launch_bounds__(512, 2) gm_kernel(Params p) {
    extern __shared__ __attribute__((aligned(16))) char smem_raw[];
    lptr smem = (lptr)smem_raw;
    const int b = blockIdx.x, tid = threadIdx.x;
    const int idx = (b >> 3) * 4 + (b & 3);
    if (b & 4) b_role(p, idx, smem, tid);
    else if (idx < 64) head_role(p, idx, smem, tid);
    else if (idx < 96) px_role(p, idx - 64, smem, tid);
    else fo_role(p, idx - 96, smem, tid);
}

extern "C" void kernel_launch(void* const* d_in, const int* in_sizes, int n_in, void* d_out, int out_size, void* d_ws, size_t ws_size, hipStream_t stream) {
    Params p;
    p.x = (const u16*)d_in[0]; p.mem0 = (const u16*)d_in[1]; p.Wq = (const u16*)d_in[2]; p.Wk = (const u16*)d_in[3]; p.Wv = (const u16*)d_in[4]; p.Wo = (const u16*)d_in[5];
    p.wqkv = (const u16*)d_in[8]; p.wo = (const u16*)d_in[9]; p.gw = (const u16*)d_in[10];
    p.out = (u16*)d_out;
    p.ctrl = (unsigned*)d_ws;
    const size_t a = ((size_t)d_ws + CTRL_BYTES + (2u << 20) - 1) & ~(size_t)((2u << 20) - 1);
    p.exch = (char*)a;
    p.big = (char*)a + (2u << 20);
    (void)hipFuncSetAttribute((const void*)gm_kernel, hipFuncAttributeMaxDynamicSharedMemorySize, LDS_BYTES);
    (void)hipMemsetAsync(d_ws, 0, CTRL_BYTES, stream);
    hipLaunchKernelGGL(gm_kernel, dim3(256), dim3(512), LDS_BYTES, stream, p);
}
```

```cpp
#include <hip/hip_runtime.h>
#include <hip/hip_bf16.h>
#include <stdint.h>

typedef unsigned short u16;
typedef __attribute__((ext_vector_type(2))) float f32x2;
typedef __attribute__((ext_vector_type(4))) float f32x4;
typedef __attribute__((ext_vector_type(16))) float f32x16;
typedef __attribute__((ext_vector_type(2))) unsigned u32x2;
typedef __attribute__((ext_vector_type(4))) unsigned u32x4;
typedef __attribute__((ext_vector_type(8))) unsigned u32x8;
typedef __attribute__((ext_vector_type(2))) __bf16 bf16x2;
typedef __attribute__((ext_vector_type(4))) __bf16 bf16x4;
typedef __attribute__((ext_vector_type(8))) __bf16 bf16x8;
typedef __attribute__((ext_vector_type(16))) __bf16 bf16x16;
typedef __attribute__((ext_vector_type(4))) short s16x4;
#define LAS __attribute__((address_space(3)))
#define GAS __attribute__((address_space(1)))
#define DI __device__ __forceinline__
typedef LAS char* lptr;

constexpr int NSEG = 64;
constexpr int LDS_BYTES = 160 * 1024;
constexpr unsigned SPIN_CAP = 1u << 18;

constexpr int L_NEAR = 0, L_INIT = 1, L_TMO = 2, L_PXP = 8, L_HXP = 40, L_FOP = 104, L_BQC = 136, L_BAC = 148, L_BOC = 152, L_END = 184;
constexpr size_t CTRL_BYTES = (size_t)L_END * 256;
constexpr unsigned X_PART = 0, X_MEM = 512 * 1024;
constexpr unsigned X_TS = 0;
constexpr unsigned KQV_K = 0, KQV_V = 9216, KQV_Q = 17408, KQV_SZ = 26624;
constexpr size_t O_KQV = 0;
constexpr size_t O_OX = O_KQV + (size_t)64 * 32 * KQV_SZ;
constexpr size_t O_FP = O_OX + (size_t)2048 * 4096 * 2;
constexpr size_t O_QB = O_FP + (size_t)64 * 32 * 16384;
constexpr size_t O_KB = O_QB + (size_t)32 * 65536;
constexpr size_t O_VB = O_KB + (size_t)32 * 65536;
constexpr size_t O_OB = O_VB + (size_t)32 * 65536;
constexpr size_t O_BP = O_OB + (size_t)32 * 65536;
constexpr size_t O_G = O_BP + (size_t)2048 * 512 * 4;
constexpr size_t BIG_BYTES = O_G + 2048 * 4;

struct Params {
    const u16* x; const u16* mem0; const u16* Wq; const u16* Wk; const u16* Wv; const u16* Wo;
    const u16* wqkv; const u16* wo; const u16* gw;
    u16* out; unsigned* ctrl; char* exch; char* big;
};

#define TSTAMP(slot)
#define PH_INIT
#define PH_ARGS
#define PH_PASS
#define PH(k)
#define PHC(k, v)
#define EV(slot)
#define EV_INIT
#define EV_OUT
#define DUR_B1
#define DUR_P3
#define DUR_INIT
#define DUR_OUT
#define PH_OUT
DI unsigned rfl(unsigned v) { return __builtin_amdgcn_readfirstlane(v); }
DI void bar() { asm volatile("s_waitcnt lgkmcnt(0)\n\ts_barrier" ::: "memory"); }
DI void vm0() { asm volatile("s_waitcnt vmcnt(0)" ::: "memory"); }
template <int N> DI void vmn() { asm volatile("s_waitcnt vmcnt(%0)" :: "n"(N) : "memory"); }
DI unsigned pk2(float a, float b) { f32x2 v = {a, b}; return __builtin_bit_cast(unsigned, __builtin_convertvector(v, bf16x2)); }
DI float bf_lo(unsigned u) { return __uint_as_float(u << 16); }
DI float bf_hi(unsigned u) { return __uint_as_float(u & 0xFFFF0000u); }
DI u32x4 lds_r16(lptr p) { return *(LAS const u32x4*)p; }
DI u32x2 lds_r8(lptr p) { return *(LAS const u32x2*)p; }
DI void lds_w16(lptr p, u32x4 v) { *(LAS u32x4*)p = v; }
DI void lds_w8(lptr p, u32x2 v) { *(LAS u32x2*)p = v; }
DI u32x2 lds_tr(lptr p) { return __builtin_bit_cast(u32x2, __builtin_amdgcn_ds_read_tr16_b64_v4bf16((LAS bf16x4*)p)); }
DI unsigned lds_addr(lptr p) { return (unsigned)(size_t)p; }
DI f32x4 mfma16(u32x4 a, u32x4 b, f32x4 c) { return __builtin_amdgcn_mfma_f32_16x16x32_bf16(__builtin_bit_cast(bf16x8, a), __builtin_bit_cast(bf16x8, b), c, 0, 0, 0); }
DI f32x16 mfma32(u32x4 a, u32x4 b, f32x16 c) { return __builtin_amdgcn_mfma_f32_32x32x16_bf16(__builtin_bit_cast(bf16x8, a), __builtin_bit_cast(bf16x8, b), c, 0, 0, 0); }
DI f32x4 smf(u32x4 a, u32x8 b, f32x4 c, int idx) { return __builtin_amdgcn_smfmac_f32_16x16x64_bf16(__builtin_bit_cast(bf16x8, a), __builtin_bit_cast(bf16x16, b), c, idx, 0, 0); }
DI u32x4 lo4(u32x8 w) { return __builtin_shufflevector(w, w, 0, 1, 2, 3); }
DI u32x4 hi4(u32x8 w) { return __builtin_shufflevector(w, w, 4, 5, 6, 7); }
DI float fold32(float a, float b) { auto r = __builtin_amdgcn_permlane32_swap(__float_as_uint(a), __float_as_uint(b), false, false); return __uint_as_float(r[0]) + __uint_as_float(r[1]); }
DI float fold16(float a, float b) { auto r = __builtin_amdgcn_permlane16_swap(__float_as_uint(a), __float_as_uint(b), false, false); return __uint_as_float(r[0]) + __uint_as_float(r[1]); }
DI float max32(float a) { auto r = __builtin_amdgcn_permlane32_swap(__float_as_uint(a), __float_as_uint(a), false, false); return fmaxf(__uint_as_float(r[0]), __uint_as_float(r[1])); }
DI float max16(float a) { auto r = __builtin_amdgcn_permlane16_swap(__float_as_uint(a), __float_as_uint(a), false, false); return fmaxf(__uint_as_float(r[0]), __uint_as_float(r[1])); }
DI float ex2(float x) { return __builtin_amdgcn_exp2f(x); }

typedef __amdgpu_buffer_rsrc_t rsrc_t;
DI rsrc_t mkrs(const void* p, unsigned bytes) { return __builtin_amdgcn_make_buffer_rsrc((void*)p, 0, bytes, 0x00020000); }
DI u32x4 ld16_dev(rsrc_t r, unsigned off) { return __builtin_amdgcn_raw_buffer_load_b128(r, off, 0, 16); }
DI void st16_dev(rsrc_t r, unsigned off, u32x4 v) { __builtin_amdgcn_raw_buffer_store_b128(v, r, off, 0, 16); }
DI void st8_dev(rsrc_t r, unsigned off, u32x2 v) { __builtin_amdgcn_raw_buffer_store_b64(v, r, off, 0, 16); }
DI void st4_dev(rsrc_t r, unsigned off, unsigned v) { __builtin_amdgcn_raw_buffer_store_b32(v, r, off, 0, 16); }
DI u32x4 ld16_dev(rsrc_t r, unsigned off, unsigned soff) { return __builtin_amdgcn_raw_buffer_load_b128(r, off, soff, 16); }
DI void st16_dev(rsrc_t r, unsigned off, unsigned soff, u32x4 v) { __builtin_amdgcn_raw_buffer_store_b128(v, r, off, soff, 16); }
DI void st8_dev(rsrc_t r, unsigned off, unsigned soff, u32x2 v) { __builtin_amdgcn_raw_buffer_store_b64(v, r, off, soff, 16); }
DI void st4_dev(rsrc_t r, unsigned off, unsigned soff, unsigned v) { __builtin_amdgcn_raw_buffer_store_b32(v, r, off, soff, 16); }
DI int opaque(int v) { asm volatile("" : "+v"(v)); return v; }
DI unsigned ldw_dev(const unsigned* p) { return __hip_atomic_load((GAS const unsigned*)p, __ATOMIC_RELAXED, __HIP_MEMORY_SCOPE_AGENT); }
DI void stw_dev(unsigned* p, unsigned v) { __hip_atomic_store((GAS unsigned*)p, v, __ATOMIC_RELAXED, __HIP_MEMORY_SCOPE_AGENT); }
DI void addw_dev(unsigned* p, unsigned v) { __hip_atomic_fetch_add((GAS unsigned*)p, v, __ATOMIC_RELAXED, __HIP_MEMORY_SCOPE_AGENT); }
DI void dma16(const void* gsrc, unsigned lds_dst) {
    unsigned keep;
    asm volatile("s_mov_b32 %0, m0\n\ts_mov_b32 m0, %2\n\ts_nop 0\n\tglobal_load_lds_dwordx4 %1, off\n\ts_mov_b32 m0, %0" : "=&s"(keep) : "v"(gsrc), "s"(lds_dst) : "memory");
}
DI void dma16_dev(const void* gsrc, unsigned lds_dst) {
    unsigned keep;
    asm volatile("s_mov_b32 %0, m0\n\ts_mov_b32 m0, %2\n\ts_nop 0\n\tglobal_load_lds_dwordx4 %1, off sc1\n\ts_mov_b32 m0, %0" : "=&s"(keep) : "v"(gsrc), "s"(lds_dst) : "memory");
}
DI void dmab16_dev(rsrc_t r, unsigned voff, unsigned soff, unsigned lds_dst) {
    unsigned keep;
    asm volatile("s_mov_b32 %0, m0\n\ts_mov_b32 m0, %4\n\ts_nop 0\n\tbuffer_load_dwordx4 %1, %2, %3 offen sc1 lds\n\ts_mov_b32 m0, %0" : "=&s"(keep) : "v"(voff), "s"(r), "s"(soff), "s"(lds_dst) : "memory");
}
DI void pin(lptr& q) { asm volatile("" : "+v"(q)); }
DI void pinu(unsigned& q) { asm volatile("" : "+v"(q)); }
DI bool spin_fail(unsigned& spins, unsigned* ctrl) {
    if (++spins > SPIN_CAP) { stw_dev(ctrl + L_TMO * 64, 1u); return true; }
    return (spins & 1023u) == 0u && rfl(ldw_dev(ctrl + L_TMO * 64)) != 0u;
}
template <int SLP = 4> DI unsigned wait_words(unsigned* ctrl, int line0, int n, unsigned want, int lane) {
    const unsigned* w = ctrl + (line0 + (lane < n ? lane : 0)) * 64;
    for (unsigned spins = 0;;) {
        const unsigned v = ldw_dev(w);
        if (__all(v >= want)) {
            unsigned m = v;
            for (int d = 1; d < n; d <<= 1) m = min(m, (unsigned)__shfl_xor((int)m, d));
            return rfl(m);
        }
        if (spin_fail(spins, ctrl)) return 0;
        __builtin_amdgcn_s_sleep(SLP);
    }
}
DI unsigned xphys(unsigned off, unsigned c) { return (off >> 13) * 16384u + c * 8192u + (off & 8191u); }

DI unsigned swz_f(unsigned row) { return (row & 1u) | ((((row >> 1) ^ (row >> 2)) & 1u) << 1) | (((row >> 3) & 1u) << 2); }
DI void stage_half(const u16* src, unsigned row_stride_b, unsigned buf, int w, int lane) {
#pragma unroll
    for (int n = 0; n < 8; ++n) {
        const unsigned i = w * 8 + n, row = 4 * i + (lane >> 4), ch = lane & 15;
        const unsigned sch = (((ch >> 1) ^ swz_f(row)) << 1) | (ch & 1);
        dma16((const char*)src + (size_t)row * row_stride_b + sch * 16, buf + i * 1024);
    }
}
template <bool SPARSE> DI unsigned frag_row(int u, int lane) {
    const unsigned gb = lane >> 4, q = (lane >> 2) & 3;
    return SPARSE ? 32 * (u >> 1) + 16 * (gb >> 1) + 8 * (q >> 1) + 4 * (gb & 1) + 2 * (u & 1) + (q & 1)
                  : 32 * (u >> 1) + 8 * gb + 4 * (u & 1) + q;
}
template <bool SPARSE> DI u32x8 read_frag(lptr buf, int row0, int pair, int lane) {
    u32x8 r;
#pragma unroll
    for (int u = 0; u < 4; ++u) {
        const unsigned row = row0 + frag_row<SPARSE>(u, lane);
        const u32x2 t = lds_tr(buf + row * 256 + ((pair ^ swz_f(row)) * 32) + (lane & 3) * 8);
        r[2 * u] = t.x; r[2 * u + 1] = t.y;
    }
    return r;
}
template <bool SPARSE> DI void load_slice(u32x8 (&W)[8], const u16* src, unsigned row_stride_b, lptr smem, unsigned bufA, unsigned bufB, int w, int lane) {
    const unsigned base = lds_addr(smem);
    stage_half(src, row_stride_b, base + bufA, w, lane);
    stage_half(src + (size_t)256 * (row_stride_b / 2), row_stride_b, base + bufB, w, lane);
    vmn<8>(); bar();
#pragma unroll
    for (int kk = 0; kk < 4; ++kk) W[kk] = read_frag<SPARSE>(smem + bufA, 64 * kk, w, lane);
    vm0(); bar();
#pragma unroll
    for (int kk = 0; kk < 4; ++kk) W[4 + kk] = read_frag<SPARSE>(smem + bufB, 64 * kk, w, lane);
    bar();
}
template <bool SPARSE> DI void load_slice2(u32x8 (&WA)[8], u32x8 (&WB)[8], bool take, int wv, const u16* src, unsigned row_stride_b, lptr smem, unsigned bufA, unsigned bufB, int w, int lane) {
    const unsigned base = lds_addr(smem);
    stage_half(src, row_stride_b, base + bufA, w, lane);
    stage_half(src + (size_t)256 * (row_stride_b / 2), row_stride_b, base + bufB, w, lane);
    vmn<8>(); bar();
    if (take) {
#pragma unroll
        for (int kk = 0; kk < 4; ++kk) { WA[kk] = read_frag<SPARSE>(smem + bufA, 64 * kk, 2 * wv, lane); WB[kk] = read_frag<SPARSE>(smem + bufA, 64 * kk, 2 * wv + 1, lane); }
    }
    vm0(); bar();
    if (take) {
#pragma unroll
        for (int kk = 0; kk < 4; ++kk) { WA[4 + kk] = read_frag<SPARSE>(smem + bufB, 64 * kk, 2 * wv, lane); WB[4 + kk] = read_frag<SPARSE>(smem + bufB, 64 * kk, 2 * wv + 1, lane); }
    }
    bar();
}
template <bool DEV> DI void stage_rows(const char* src, size_t row_stride_b, unsigned buf, int w, int lane) {
#pragma unroll
    for (int n = 0; n < 4; ++n) {
        const unsigned row = 4 * w + n, c = (lane & ~15) | ((lane ^ row) & 15);
        if (DEV) dma16_dev(src + row * row_stride_b + c * 16, buf + row * 1024);
        else dma16(src + row * row_stride_b + c * 16, buf + row * 1024);
    }
}
DI lptr afrag_ptr(lptr tile, int ks, int lane) { const int row = lane & 15, c = 4 * ks + (lane >> 4); return tile + row * 1024 + ((c & ~15) | ((c ^ row) & 15)) * 16; }
DI u32x4 wfrag(const u32x8 (&W)[8], int ks) { return (ks & 1) ? hi4(W[ks >> 1]) : lo4(W[ks >> 1]); }

constexpr int H_WO = 0;
constexpr int H_KX = 65536;
constexpr int H_KX_SZ = 22528;
constexpr int H_MEM = H_KX + 2 * H_KX_SZ;
constexpr int H_QT = H_MEM + 8192;
constexpr int H_KT = H_QT + 2048;
constexpr int H_VT = H_KT + 2048;
constexpr int H_OI = H_VT + 2048;
constexpr int H_ZERO = H_OI + 2304;
constexpr int H_CNT = H_ZERO + 16;
constexpr int H_GO = H_CNT + 8;
constexpr int H_LAG = H_CNT + 12;
constexpr int H_WV7 = (H_CNT + 16 + 1023) / 1024 * 1024;
constexpr int H_END = H_WV7 + 32768;
static_assert(H_END + 1024 <= LDS_BYTES, "");
#define PROBE_N 3
#define POLL_MODE 0
#define MG_PAUSE_N 1
#define RG_PAUSE_N 1
#define STR2(x) #x
#define STR(x) STR2(x)
#define MG_PAUSE "s_sleep " STR(MG_PAUSE_N) "\n\t"
#define RG_PAUSE "s_sleep " STR(RG_PAUSE_N) "\n\t"
constexpr int POLL_CAP = 1 << 17;
#define PRIO_A 1
#define WAIT_RED 35
#define WAIT_MEM 30
#define LAG_SHIFT 1
DI unsigned tick() { return (unsigned)__builtin_amdgcn_s_memrealtime(); }
DI void wait_tick(unsigned t) { for (int n = 0; n < 4096 && (int)(tick() - t) < 0; ++n) {} }
#define KX_AT 2
#define PRETOUCH 0
#define PRIO_P 1
#define PRIO_V 2
#define SLEEP_RED 14
#define SLEEP_MEM23 40
#define SLEEP_MEM 5
constexpr float SCL_A = 0.08838834764831845f * 1.4426950408889634f;

DI unsigned umax4(u32x4 v) { return max(max(v[0], v[1]), max(v[2], v[3])); }
DI unsigned red_bad(u32x4 a, u32x4 b, u32x4 c, u32x4 d, unsigned tag) {
    const u32x4 x = ((a ^ tag) | (b ^ tag)) | ((c ^ tag) | (d ^ tag));
    return (x.x | x.y) | (x.z | x.w);
}
DI float max12(f32x4 a, f32x4 b, f32x4 c) {
    float r, t;
    asm("s_nop 7\n\tv_max3_f32 %0, %2, %3, %4\n\tv_max3_f32 %1, %5, %6, %7\n\tv_max3_f32 %0, %0, %8, %9\n\tv_max3_f32 %1, %1, %10, %11\n\t"
        "v_max3_f32 %0, %0, %12, %13\n\tv_max_f32 %0, %0, %1"
        : "=&v"(r), "=&v"(t)
        : "v"(a[0]), "v"(a[1]), "v"(a[2]), "v"(a[3]), "v"(b[0]), "v"(b[1]), "v"(b[2]), "v"(b[3]), "v"(c[0]), "v"(c[1]), "v"(c[2]), "v"(c[3]));
    return r;
}
DI float vmax2(unsigned a, unsigned b) { float r; asm("v_max_f32 %0, %1, %2" : "=v"(r) : "v"(a), "v"(b)); return r; }
DI float xmax16(float a) { auto r = __builtin_amdgcn_permlane16_swap(__float_as_uint(a), __float_as_uint(a), false, false); return vmax2(r[0], r[1]); }
DI float xmax32(float a) { auto r = __builtin_amdgcn_permlane32_swap(__float_as_uint(a), __float_as_uint(a), false, false); return vmax2(r[0], r[1]); }
DI void load_kx(lptr smem, int kx, int lane, u32x4 (&k1)[4], u32x4 (&k2)[4]) {
    lptr ptr_k = smem + kx + (lane & 15) * 272 + (lane >> 4) * 16;
#pragma unroll
    for (int ks = 0; ks < 4; ++ks) { k1[ks] = lds_r16(ptr_k + ks * 64); k2[ks] = lds_r16(ptr_k + ks * 64 + 16 * 272); }
}
template <int N> struct IC { static constexpr int value = N; };
struct AttAddr { lptr t, q, vx, vm, vm1; };
DI AttAddr att_addr(lptr smem, int kx, int wv, int lane) {
    const int c16 = lane & 15, g = lane >> 4, e = 32 * wv + c16;
    AttAddr a;
    a.t = smem + H_QT + (8 * g + ((lane >> 2) & 3)) * 16 + (lane & 1) * 8;
    a.q = smem + kx + 17408 + c16 * 272 + g * 16;
    a.vx = smem + kx + 9216 + e * 64 + ((g ^ ((4 - ((e >> 2) & 3)) & 3)) * 16);
    a.vm = smem + (g < 2 ? H_VT + e * 16 + g * 8 : H_ZERO);
    a.vm1 = a.vm + (g < 2 ? 256 : 0);
    return a;
}
DI void pin(AttAddr& a) { pin(a.t); pin(a.q); pin(a.vx); pin(a.vm); pin(a.vm1); }
template <bool XQ, bool VBAR> DI void head_attn(AttAddr ad, int half, int lane, const u32x4 (&k1)[4], const u32x4 (&k2)[4], f32x4 (&o)[2], float& rl PH_ARGS) {
    const int c16 = lane & 15, g = lane >> 4;
    u32x4 qf[4], k0[4];
#pragma unroll
    for (int ks = 0; ks < 4; ++ks) {
        if (XQ) qf[ks] = lds_r16(ad.q + ks * 64);
        else { const u32x2 a = lds_tr(ad.t + ks * 512), b = lds_tr(ad.t + ks * 512 + 64); qf[ks] = (u32x4){a.x, a.y, b.x, b.y}; }
        const u32x2 ka = lds_tr(ad.t + 2048 + ks * 512), kb = lds_tr(ad.t + 2048 + ks * 512 + 64);
        k0[ks] = (u32x4){ka.x, ka.y, kb.x, kb.y};
    }
    __builtin_amdgcn_sched_barrier(0);
    f32x4 s0 = {0, 0, 0, 0}, s1 = {0, 0, 0, 0}, s2 = {0, 0, 0, 0};
#pragma unroll
    for (int ks = 0; ks < 4; ++ks) { s1 = mfma16(k1[ks], qf[ks], s1); s2 = mfma16(k2[ks], qf[ks], s2); s0 = mfma16(k0[ks], qf[ks], s0); }
    const u32x4 vx0 = lds_r16(ad.vx), vx1 = lds_r16(ad.vx + 1024);
    if (XQ) {
        const float NEG = -__builtin_inff();
        const int qi = 16 * half + c16 - 4 * g;
#pragma unroll
        for (int r = 0; r < 4; ++r) { if (r > qi) s1[r] = NEG; if (16 + r > qi) s2[r] = NEG; }
    }
    const float m = xmax32(xmax16(max12(s0, s1, s2)));
    float mc = m * -SCL_A;
    if (VBAR) asm volatile("s_waitcnt lgkmcnt(0)\n\ts_barrier" : "+v"(mc) :: "memory");
    const u32x2 vm0_ = lds_r8(ad.vm), vm1_ = lds_r8(ad.vm1);
#pragma unroll
    for (int r = 0; r < 4; ++r) { s1[r] = ex2(fmaf(s1[r], SCL_A, mc)); s2[r] = ex2(fmaf(s2[r], SCL_A, mc)); }
    const u32x4 px = {pk2(s1[0], s1[1]), pk2(s1[2], s1[3]), pk2(s2[0], s2[1]), pk2(s2[2], s2[3])};
    f32x4 a0 = {0, 0, 0, 0}, a1 = {0, 0, 0, 0};
    a0 = mfma16(vx0, px, a0); a1 = mfma16(vx1, px, a1);
#pragma unroll
    for (int r = 0; r < 4; ++r) s0[r] = ex2(fmaf(s0[r], SCL_A, mc));
    const u32x4 p0 = {pk2(s0[0], s0[1]), pk2(s0[2], s0[3]), 0u, 0u};
    o[0] = mfma16((u32x4){vm0_.x, vm0_.y, 0u, 0u}, p0, a0);
    o[1] = mfma16((u32x4){vm1_.x, vm1_.y, 0u, 0u}, p0, a1);
    float l0 = (s0[0] + s0[1]) + (s0[2] + s0[3]);
    float l = ((s1[0] + s1[1]) + (s1[2] + s1[3])) + ((s2[0] + s2[1]) + (s2[2] + s2[3]));
    l += g < 2 ? l0 : 0.0f;
    l = fold16(l, l); l = fold32(l, l);
    rl = __builtin_amdgcn_rcpf(l);
}

DI void head_role(const Params& p, int i, lptr smem, int tid_) {
    const int tid = tid_, lane = tid & 63, w = rfl(tid >> 6);
    const int h = (i >> 3) * 4 + (i & 3), half = (i >> 2) & 1;
    const unsigned lbase = lds_addr(smem);
    unsigned* ctrl = p.ctrl;
    const rsrc_t rx = mkrs(p.exch, 2u << 20);
    if (i == 0 && tid == 0) {
        unsigned best0 = ~0u, best1 = ~0u;
        for (int k = 0; k < 2 * PROBE_N; ++k) {
            unsigned* q = (unsigned*)(p.exch + (k & 1) * 8192);
            unsigned xx = k;
            asm volatile("global_store_dword %0, %1, off sc1\n\ts_waitcnt vmcnt(0)" :: "v"(q), "v"(xx) : "memory");
            const unsigned long long t0 = __builtin_amdgcn_s_memtime();
            asm volatile("global_load_dword %0, %1, off sc1\n\ts_waitcnt vmcnt(0)" : "=v"(xx) : "v"(q) : "memory");
            const unsigned t = (unsigned)(__builtin_amdgcn_s_memtime() - t0);
            if (k & 1) best1 = min(best1, t); else best0 = min(best0, t);
        }
        stw_dev(ctrl + L_NEAR * 64, best0 <= best1 ? 1u : 2u);
    }
    TSTAMP(0);
    const int wv = w & 3;
    u32x8 WA[8], WB[8], WK[8];
    load_slice<true>(WK, p.Wk + h * 128, 8192, smem, 65536, 0, w, lane);
    load_slice2<true>(WA, WB, w < 4, wv, p.Wq + h * 128, 8192, smem, 65536, 0, w, lane);
    load_slice2<true>(WA, WB, w >= 4, wv, p.Wv + h * 128, 8192, smem, 65536, 0, w, lane);
    {
        const u16* src = p.Wo + (size_t)h * 128 * 512 + half * 256;
#pragma unroll
        for (int n = 0; n < 8; ++n) {
            const unsigned ii = w * 8 + n, sub = ii >> 5, row = 4 * (ii & 31) + (lane >> 4), ch = lane & 15;
            const unsigned sch = (((ch >> 1) ^ swz_f(row)) << 1) | (ch & 1);
            dma16((const char*)src + (size_t)row * 1024 + sub * 256 + sch * 16, lbase + 65536 + ii * 1024);
        }
        vm0(); bar();
#pragma unroll
        for (int t = 0; t < 2; ++t)
#pragma unroll
            for (int ks = 0; ks < 2; ++ks) {
                const int col16 = 2 * w + t;
                const u32x8 f = read_frag<true>(smem + 65536 + (col16 >> 3) * 32768, 64 * ks, col16 & 7, lane);
                lptr d = smem + H_WO + ((w * 2 + t) * 2 + ks) * 2048 + lane * 16;
                lds_w16(d, lo4(f)); lds_w16(d + 1024, hi4(f));
            }
        bar();
    }
    lds_w16(smem + H_WV7 + w * 4096 + lane * 16, lo4(WB[6])); lds_w16(smem + H_WV7 + w * 4096 + 1024 + lane * 16, hi4(WB[6]));
    lds_w16(smem + H_WV7 + w * 4096 + 2048 + lane * 16, lo4(WB[7])); lds_w16(smem + H_WV7 + w * 4096 + 3072 + lane * 16, hi4(WB[7]));
    TSTAMP(1);
    const unsigned nearw = wait_words(ctrl, L_NEAR, 1, 1, lane);
    if (!nearw) return;
    const unsigned cn = nearw - 1;
    st16_dev(rx, xphys(X_PART + i * 8192 + tid * 16, cn), (u32x4){0u, 0u, 0u, 0u});
    {
        u32x4 v = {~0u, ~0u, ~0u, ~0u};
        if (tid < 8) {
            v = *(const GAS u32x4*)(p.mem0 + tid * 512 + i * 8);
#pragma unroll
            for (int r = 0; r < 4; ++r) if (v[r] == ~0u) v[r] = 0x7FFF7FFFu;
        }
        st16_dev(rx, xphys(X_MEM + (tid >> 3) * 8192 + i * 128 + (tid & 7) * 16, cn), v);
    }
    if (tid < 64) st4_dev(rx, xphys(X_TS + tid * 8192 + i * 128, cn ^ 1), 0u);
    vm0(); bar();
    if (tid == 0) addw_dev(ctrl + L_INIT * 64, 1u);
    unsigned px_known = wait_words(ctrl, L_PXP + h, 1, 1, lane);
    if (!px_known) return;
    const rsrc_t rkq = mkrs(p.big + O_KQV, 64u * 32u * KQV_SZ);
    auto prefetch_kx = [&](int s, int ln, int part) {
        const unsigned src = (unsigned)(s * 32 + h) * KQV_SZ;
        const unsigned dst = lbase + H_KX + (s & 1) * H_KX_SZ;
        const int v = w - 4;
        if (part == 0) {
#pragma unroll
            for (int n = 0; n < 3; ++n) dmab16_dev(rkq, ln * 16, src + (v * 3 + n) * 1024, dst + (v * 3 + n) * 1024);
        } else {
            dmab16_dev(rkq, ln * 16, src + (12 + v) * 1024, dst + (12 + v) * 1024);
            dmab16_dev(rkq, ln * 16, src + KQV_Q + half * 4352 + v * 1024, dst + 17408 + v * 1024);
            if (v == 0) dmab16_dev(rkq, ln * 16, src + 16384, dst + 16384);
            if (v == 1) dmab16_dev(rkq, ln * 16, src + KQV_Q + half * 4352 + 4096, dst + 17408 + 4096);
        }
    };
    if (w >= 4) { prefetch_kx(0, lane, 0); prefetch_kx(0, lane, 1); vm0(); }
    if (tid < 4) *(LAS unsigned*)(smem + H_CNT + tid * 4) = 0u;
    if (!wait_words(ctrl, L_INIT, 1, 64, lane)) return;
    const int sidx = (lane & 8) ? 0xEEEE : 0x4444;
    if (tid < 8) *(LAS unsigned*)(smem + H_ZERO + tid * 4) = 0u;
    TSTAMP(2);
    PH_INIT
    EV_INIT
    DUR_INIT
    const unsigned tid16 = tid * 16;

    auto gather_mem = [&](int s, u32x4& m0, u32x4& m1) -> bool {
        const unsigned soff = xphys(X_MEM + s * 8192, cn);
        int cnt = POLL_CAP; bool bad;
        do {
            asm volatile("" ::: "memory");
            m0 = ld16_dev(rx, tid16, soff); m1 = ld16_dev(rx, tid16 + 4096, soff);
            bad = __builtin_amdgcn_ballot_w64(max(umax4(m0), umax4(m1)) == ~0u) != 0;
        } while (bad && --cnt);
        if (bad) { stw_dev(ctrl + L_TMO * 64, 1u); return false; }
        PH(0)
        PHC(11, POLL_CAP - cnt)
        if (s == 31) EV(3);
        lds_w16(smem + H_MEM + tid16, m0); lds_w16(smem + H_MEM + tid16 + 4096, m1);
        return true;
    };
    auto qk_mfma = [&](auto WC, int s_, lptr ap, const u32x4 m0, const u32x4 m1, const u32x4 (&bl)[4], f32x4& qa, f32x4& qb, f32x4& ak) {
        constexpr int W = decltype(WC)::value;
        auto wb = [&](int ks) { return ks == 7 ? __builtin_shufflevector(bl[2], bl[3], 0, 1, 2, 3, 4, 5, 6, 7) : ks == 6 ? __builtin_shufflevector(bl[0], bl[1], 0, 1, 2, 3, 4, 5, 6, 7) : WB[ks]; };
        ak = smf(m0, WK[W], ak, sidx); qa = smf(m0, WA[W], qa, sidx); qb = smf(m0, wb(W), qb, sidx);
        ak = smf(m1, WK[W + 4], ak, sidx); qa = smf(m1, WA[W + 4], qa, sidx); qb = smf(m1, wb(W + 4), qb, sidx);
        bar();
        PH(1)
        if (s_ == 31) EV(4);
        DUR_B1
        u32x4 A[6];
#pragma unroll
        for (int n = 0; n < 6; ++n) { const int ks = (W + 1 + n + (n >= 3 ? 1 : 0)) & 7; A[n] = lds_r16(ap + ks * 1024); }
        __builtin_amdgcn_sched_barrier(0);
#pragma unroll
        for (int n = 0; n < 6; ++n) {
            const int ks = (W + 1 + n + (n >= 3 ? 1 : 0)) & 7;
            ak = smf(A[n], WK[ks], ak, sidx); qa = smf(A[n], WA[ks], qa, sidx); qb = smf(A[n], wb(ks), qb, sidx);
            __builtin_amdgcn_sched_barrier(0);
        }
    };
    auto qk_lo = [&](int s_, const u32x4 m0, const u32x4 m1, const u32x4 (&bl)[4], u32x4 (&k1)[4], u32x4 (&k2)[4]) {
        const int ln = opaque(lane);
        lptr ap = smem + H_MEM + ln * 16;
        lptr wq = smem + H_QT + (32 * w + 16 * (ln >> 5) + (ln & 15)) * 16 + ((ln >> 4) & 1) * 8;
        lptr wk = smem + H_KT + (16 * w + (ln & 15)) * 16 + ((ln >> 4) & 1) * 8;
        pin(ap); pin(wq); pin(wk);
        f32x4 qa = {0, 0, 0, 0}, qb = {0, 0, 0, 0}, ak = {0, 0, 0, 0};
        if (w == 0) qk_mfma(IC<0>{}, s_, ap, m0, m1, bl, qa, qb, ak);
        else if (w == 1) qk_mfma(IC<1>{}, s_, ap, m0, m1, bl, qa, qb, ak);
        else if (w == 2) qk_mfma(IC<2>{}, s_, ap, m0, m1, bl, qa, qb, ak);
        else qk_mfma(IC<3>{}, s_, ap, m0, m1, bl, qa, qb, ak);
        __builtin_amdgcn_sched_barrier(0);
        load_kx(smem, H_KX + (s_ & 1) * H_KX_SZ, ln, k1, k2);
        const float g0 = fold32(ak[0], ak[0]), g1 = fold32(ak[1], ak[1]), g2 = fold32(ak[2], ak[2]), g3 = fold32(ak[3], ak[3]);
        if (ln < 32) lds_w8(wk, (u32x2){pk2(g0, g1), pk2(g2, g3)});
        const float f0 = fold32(qa[0], qb[0]), f1 = fold32(qa[1], qb[1]), f2 = fold32(qa[2], qb[2]), f3 = fold32(qa[3], qb[3]);
        lds_w8(wq, (u32x2){pk2(f0, f1), pk2(f2, f3)});
        PH(2)
    };
    auto publish_ = [&](int done) {
        unsigned old = 0;
        if (lane == 0) old = __hip_atomic_fetch_add((LAS unsigned*)(smem + H_CNT + (done & 1) * 4), 1u, __ATOMIC_RELAXED, __HIP_MEMORY_SCOPE_WORKGROUP);
        if (lane == 0 && (old & 3u) == 3u) stw_dev(ctrl + L_HXP * 64 + i * 64, done);
    };
    auto kv_hi = [&](int s_) -> bool {
        const int ln = opaque(lane);
        lptr ap = smem + H_MEM + ln * 16;
        lptr wk = smem + H_KT + (16 * w + (ln & 15)) * 16 + ((ln >> 4) & 1) * 8;
        lptr vp = smem + H_WV7 + w * 4096 + ln * 16;
        lptr wvp = smem + H_VT + (32 * wv + 16 * (ln >> 5) + (ln & 15)) * 16 + ((ln >> 4) & 1) * 8;
        pin(ap); pin(wk); pin(vp); pin(wvp);
        bar();
        PH(1)
        u32x4 A[8];
#pragma unroll
        for (int ks = 0; ks < 8; ++ks) A[ks] = lds_r16(ap + ks * 1024);
        __builtin_amdgcn_sched_barrier(0);
        f32x4 ak = {0, 0, 0, 0};
#pragma unroll
        for (int ks = 0; ks < 8; ++ks) ak = smf(A[ks], WK[ks], ak, sidx);
        const float g0 = fold32(ak[0], ak[0]), g1 = fold32(ak[1], ak[1]), g2 = fold32(ak[2], ak[2]), g3 = fold32(ak[3], ak[3]);
        if (ln < 32) lds_w8(wk, (u32x2){pk2(g0, g1), pk2(g2, g3)});
        PH(2)
        bar();
        PH(3)
        __builtin_amdgcn_s_setprio(PRIO_V);
        f32x4 va = {0, 0, 0, 0}, vb = {0, 0, 0, 0};
        u32x4 b6a, b6b, b7a, b7b;
#pragma unroll
        for (int ks = 0; ks < 8; ++ks) {
            va = smf(A[ks], WA[ks], va, sidx);
            vb = smf(A[ks], ks == 7 ? __builtin_shufflevector(b7a, b7b, 0, 1, 2, 3, 4, 5, 6, 7) : ks == 6 ? __builtin_shufflevector(b6a, b6b, 0, 1, 2, 3, 4, 5, 6, 7) : WB[ks], vb, sidx);
            if (ks == 1) { b6a = lds_r16(vp); b6b = lds_r16(vp + 1024); }
            if (ks == 3) { b7a = lds_r16(vp + 2048); b7b = lds_r16(vp + 3072); }
            __builtin_amdgcn_sched_barrier(0);
        }
        const float f0 = fold32(va[0], vb[0]), f1 = fold32(va[1], vb[1]), f2 = fold32(va[2], vb[2]), f3 = fold32(va[3], vb[3]);
        lds_w8(wvp, (u32x2){pk2(f0, f1), pk2(f2, f3)});
        __builtin_amdgcn_s_setprio(0);
        bar();
        return true;
    };
    auto proj = [&](int s, const u32x4 (&wo)[8]) {
        const int ln = opaque(lane);
        lptr op = smem + H_OI + (ln & 7) * 272 + (16 * (ln >> 4) + 8 * ((ln >> 3) & 1)) * 2;
        unsigned so = (2 * (ln >> 5) + ((ln & 15) >> 3)) * 16384 + ((ln >> 4) & 1) * 4096 + (ln & 7) * 16;
        pin(op); pinu(so);
        bar();
        PH(5)
        const unsigned tag = (s & 1) ^ 1;
        const u32x4 oa = lds_r16(op), ob = lds_r16(op + 128);
        f32x4 a0 = {0, 0, 0, 0}, a1 = {0, 0, 0, 0};
        a0 = smf(oa, __builtin_shufflevector(wo[0], wo[1], 0, 1, 2, 3, 4, 5, 6, 7), a0, sidx);
        a1 = smf(oa, __builtin_shufflevector(wo[4], wo[5], 0, 1, 2, 3, 4, 5, 6, 7), a1, sidx);
        a0 = smf(ob, __builtin_shufflevector(wo[2], wo[3], 0, 1, 2, 3, 4, 5, 6, 7), a0, sidx);
        a1 = smf(ob, __builtin_shufflevector(wo[6], wo[7], 0, 1, 2, 3, 4, 5, 6, 7), a1, sidx);
        u32x4 pv;
#pragma unroll
        for (int r = 0; r < 4; ++r) pv[r] = (__float_as_uint(fold32(a0[r], a1[r])) & ~1u) | tag;
        st16_dev(rx, so, xphys(X_PART + (32 * half + 4 * w) * 8192 + h * 128, cn), pv);
        PH(6)
        if (s == 30) EV(0);
    };

    if (w < 4) {
        const int W = w;
        __builtin_amdgcn_s_setprio(PRIO_A);
        unsigned t_ms = 0;
        for (int s = 0; s < NSEG; ++s) {
            u32x4 bl[4];
            {
                lptr vp = smem + H_WV7 + W * 4096 + opaque(lane) * 16;
                bl[0] = lds_r16(vp); bl[1] = lds_r16(vp + 1024); bl[2] = lds_r16(vp + 2048); bl[3] = lds_r16(vp + 3072);
            }
            if (s > 0) {
                if (W < 2) {
                    const unsigned lag = min(rfl(*(volatile LAS unsigned*)(smem + H_LAG)) >> LAG_SHIFT, (unsigned)WAIT_MEM);
                    wait_tick(t_ms + WAIT_MEM - lag);
                    if (W == 0 && lane == 0) *(volatile LAS unsigned*)(smem + H_GO) = s;
                } else {
                    int cnt = POLL_CAP;
                    while (rfl(*(volatile LAS unsigned*)(smem + H_GO)) != (unsigned)s && --cnt) __builtin_amdgcn_s_sleep(1);
                }
            }
            PH(12)
            if (s == 31) EV(6);
            u32x4 m0, m1;
            if (!gather_mem(s, m0, m1)) return;
            u32x4 k1[4], k2[4];
            qk_lo(s, m0, m1, bl, k1, k2);
            if (s + 1 == NSEG) { bar(); bar(); break; }
            const int ln = opaque(lane);
            AttAddr ad = att_addr(smem, H_KX + (s & 1) * H_KX_SZ, W, ln);
            const int t = (ln >> 3) & 1;
            lptr oip = smem + H_OI + (ln & 7) * 272 + (32 * W + 16 * t + 4 * (ln >> 4)) * 2;
            lptr wop = smem + H_WO + W * 8192 + ln * 16;
            pin(ad); pin(oip); pin(wop);
            bar();
            PH(3)
            u32x4 wo[8];
            {
                f32x4 o[2]; float rl;
                head_attn<false, true>(ad, half, ln, k1, k2, o, rl PH_PASS);
#pragma unroll
                for (int n = 0; n < 8; ++n) wo[n] = lds_r16(wop + n * 1024);
                const f32x4 ov = (t ? o[1] : o[0]) * rl;
                lds_w8(oip, (u32x2){pk2(ov[0], ov[1]), pk2(ov[2], ov[3])});
            }
            PH(4)
            proj(s, wo);
            const unsigned t_p3 = tick();
            DUR_P3
            if (W == 2) {
                if (s > 0) {
                    const unsigned v = __builtin_amdgcn_raw_buffer_load_b32(rx, lane * 128, xphys(X_TS + s * 8192, cn ^ 1), 16);
                    const unsigned own = __builtin_amdgcn_readlane(v, i);
                    unsigned d = own - v;
                    d = d < 200u ? d : 0u;
#pragma unroll
                    for (int m = 1; m < 64; m <<= 1) d = max(d, (unsigned)__shfl_xor((int)d, m));
                    if (lane == 0) *(volatile LAS unsigned*)(smem + H_LAG) = d;
                }
            }
            if (W < 2) {
                wait_tick(t_p3 + WAIT_RED);
                PH(13)
                if (s == 30) EV(5);
                const unsigned tag = (s & 1) ^ 1;
                const int l2 = opaque(lane);
                const unsigned soff = xphys(X_PART + i * 8192 + W * 4096, cn);
                unsigned cnt, t, t2; float s0, s1, s2, s3;
                const unsigned vo = l2 * 16;
#define RG_LD(a) "buffer_load_dwordx4 v[" #a ":" #a "+3], %[off], %[rs], %[so] offen sc1\n\tbuffer_load_dwordx4 v[" #a "+4:" #a "+7], %[off], %[rs], %[so] offen offset:1024 sc1\n\t" \
                 "buffer_load_dwordx4 v[" #a "+8:" #a "+11], %[off], %[rs], %[so] offen offset:2048 sc1\n\tbuffer_load_dwordx4 v[" #a "+12:" #a "+15], %[off], %[rs], %[so] offen offset:3072 sc1\n\t"
#define RG_X(o, a, i, j) "v_bitop3_b32 %[" #o "], v[" #a "+" #i "], v[" #a "+" #j "], %[tag] bitop3:0x7e\n\t"
#define RG_O(a, i, j) "v_bitop3_b32 %[t2], v[" #a "+" #i "], v[" #a "+" #j "], %[tag] bitop3:0x7e\n\tv_or_b32 %[t], %[t], %[t2]\n\t"
#define RG_CK(a, lab) RG_CKN(a, lab, 4)
#define RG_CKN(a, lab, n) "s_waitcnt vmcnt(" #n ")\n\t" \
                      RG_X(t, a, 0, 1) RG_X(s0, a, 2, 3) RG_X(s1, a, 4, 5) RG_X(s2, a, 6, 7) RG_X(s3, a, 8, 9) RG_X(t2, a, 10, 11) \
                      "v_or3_b32 %[t], %[t], %[s0], %[s1]\n\tv_or3_b32 %[s2], %[s2], %[s3], %[t2]\n\t" RG_X(s0, a, 12, 13) RG_X(s1, a, 14, 15) \
                      "v_or3_b32 %[t], %[t], %[s2], %[s0]\n\tv_or_b32 %[t], %[t], %[s1]\n\t" \
                      "v_and_b32 %[t], 1, %[t]\n\tv_cmp_ne_u32 vcc, 0, %[t]\n\ts_cbranch_vccz " lab "\n\t"
#define RG_A(o, a, i) "v_add_f32 %[" #o "], v[" #a "+" #i "], v[" #a "+4+" #i "]\n\tv_add_f32 %[t], v[" #a "+8+" #i "], v[" #a "+12+" #i "]\n\tv_add_f32 %[" #o "], %[" #o "], %[t]\n\t"
#define RG_SUM(a) RG_A(s0, a, 0) RG_A(s1, a, 1) RG_A(s2, a, 2) RG_A(s3, a, 3)
                asm volatile(
                    "s_mov_b32 %[cnt], %[cap]\n\t"
                    "1:\n\t"
                    RG_LD(240) RG_CKN(240, "2f", 0) RG_PAUSE
                    "s_sub_u32 %[cnt], %[cnt], 1\n\ts_cbranch_scc0 1b\n\t"
                    "s_mov_b32 %[cnt], 0\n\ts_branch 5f\n\t"
                    "2:\n\t" RG_SUM(240) "s_or_b32 %[cnt], %[cnt], 1\n\t"
                    "5:"
                    : [cnt] "=&s"(cnt), [t] "=&v"(t), [t2] "=&v"(t2), [s0] "=&v"(s0), [s1] "=&v"(s1), [s2] "=&v"(s2), [s3] "=&v"(s3)
                    : [off] "v"(vo), [rs] "s"(rx), [so] "s"(soff), [tag] "s"(tag), [cap] "n"(POLL_CAP & ~1)
                    : "memory", "vcc", "scc",
                      "v240", "v241", "v242", "v243", "v244", "v245", "v246", "v247", "v248", "v249", "v250", "v251", "v252", "v253", "v254", "v255");
#undef RG_LD
#undef RG_X
#undef RG_O
#undef RG_CK
#undef RG_CKN
#undef RG_A
#undef RG_SUM
                if (cnt == 0) { stw_dev(ctrl + L_TMO * 64, 1u); return; }
                float c = fold16(fold32(s0, s2), fold32(s1, s3));
                PH(7)
                PHC(10, (POLL_CAP & ~1) - (cnt & ~1))
                if (s == 30) EV(1);
                c += __uint_as_float(__builtin_amdgcn_update_dpp(0u, __float_as_uint(c), 0x128, 0xF, 0xF, true));
                const float cn1 = __uint_as_float(__builtin_amdgcn_update_dpp(0u, __float_as_uint(c), 0xB1, 0xF, 0xF, true));
                unsigned o2 = pk2(c, cn1);
                if (o2 == ~0u) o2 = 0x7FFF7FFFu;
                if ((l2 & 9) == 0) st4_dev(rx, (l2 >> 4) * 16 + (l2 & 6) * 2, xphys(X_MEM + (s + 1) * 8192 + i * 128 + W * 64, cn), o2);
                t_ms = tick();
                if (W == 0 && l2 == 0) st4_dev(rx, 0, xphys(X_TS + (s + 1) * 8192 + i * 128, cn ^ 1), t_ms);
                PH(8)
                if (s == 30) EV(2);
            }
        }
    } else {
        const rsrc_t rox = mkrs(p.big + O_OX, 2048u * 8192u);
        for (int s = 0; s < NSEG; ++s) {
            if (!kv_hi(s)) return;
            if (s + 1 < NSEG) {
                const int ln = opaque(lane);
                u32x4 wo[8];
                lptr wop = smem + H_WO + w * 8192 + ln * 16;
#pragma unroll
                for (int n = 0; n < 8; ++n) wo[n] = lds_r16(wop + n * 1024);
                PH(4)
                __builtin_amdgcn_s_setprio(PRIO_P);
                proj(s, wo);
                __builtin_amdgcn_s_setprio(0);
                if (s > 0) { vmn<1>(); publish_(s); }
            } else { vm0(); publish_(s); }
            if (s + 1 < NSEG) {
                const int lf = opaque(lane);
                if (px_known < (unsigned)s + 2) { px_known = wait_words(ctrl, L_PXP + h, 1, s + 2, lf); if (!px_known) return; }
                prefetch_kx(s + 1, lf, 0); prefetch_kx(s + 1, lf, 1);
            }
            const int ln = opaque(lane);
            f32x4 o[2]; float rl;
            u32x4 k1[4], k2[4];
            load_kx(smem, H_KX + (s & 1) * H_KX_SZ, ln, k1, k2);
            head_attn<true, false>(att_addr(smem, H_KX + (s & 1) * H_KX_SZ, w - 4, ln), half, ln, k1, k2, o, rl PH_PASS);
            {
                const f32x4 a = o[0] * rl, b = o[1] * rl;
                auto x = __builtin_amdgcn_permlane16_swap(pk2(a[0], a[1]), pk2(b[0], b[1]), false, false);
                auto y = __builtin_amdgcn_permlane16_swap(pk2(a[2], a[3]), pk2(b[2], b[3]), false, false);
                const unsigned g = ln >> 4;
                st16_dev(rox, (ln & 15) * 64 + ((g & 1) * 2 + (g >> 1)) * 16, (unsigned)(((s * 2 + half) * 32 + h) * 4 + (w - 4)) * 1024u, (u32x4){x[0], y[0], x[1], y[1]});
            }
            PH(9)
            if (s + 2 < NSEG && px_known < (unsigned)s + 3) px_known = max(px_known, rfl(ldw_dev(ctrl + (L_PXP + h) * 64)));
            if (s + 1 < NSEG) vmn<1>();
            else { vm0(); publish_(NSEG); }
            PH(10)
        }
    }
    TSTAMP(3);
    PH_OUT
    EV_OUT
    DUR_OUT
}

constexpr int P_X = 0;
constexpr int P_OUT = 65536;
DI void px_role(const Params& p, int h, lptr smem, int tid) {
    const int lane = tid & 63, w = rfl(tid >> 6);
    const unsigned lbase = lds_addr(smem);
    u32x8 Wq[8], Wk[8], Wv[8];
    TSTAMP(0);
    load_slice<false>(Wk, p.Wk + h * 128, 8192, smem, 65536, 0, w, lane);
    load_slice<false>(Wv, p.Wv + h * 128, 8192, smem, 65536, 0, w, lane);
    load_slice<false>(Wq, p.Wq + h * 128, 8192, smem, 65536, 0, w, lane);
    TSTAMP(1);
    const rsrc_t rk = mkrs(p.big + O_KQV, 64u * 32u * KQV_SZ);
    stage_rows<false>((const char*)p.x, 1024, lbase + P_X, w, lane);
    const int c16 = lane & 15, g = lane >> 4;
    for (int s = 0; s < NSEG; ++s) {
        if (s < 2) vm0(); else if (w < 2) vmn<4>(); else vmn<3>();
        bar();
        if (s > 0 && tid == 0) stw_dev(p.ctrl + (L_PXP + h) * 64, s == 1 ? 1 : s - 1);
        if (s == 1) TSTAMP(2);
        if (s + 1 < NSEG) stage_rows<false>((const char*)p.x + (size_t)(s + 1) * 32768, 1024, lbase + P_X + ((s + 1) & 1) * 32768, w, lane);
        lptr tile = smem + P_X + (s & 1) * 32768;
        f32x4 q0 = {0, 0, 0, 0}, q1 = q0, k0 = q0, k1 = q0, v0 = q0, v1 = q0;
#pragma unroll
        for (int ks = 0; ks < 16; ++ks) {
            lptr a = afrag_ptr(tile, ks, lane);
            const u32x4 x0 = lds_r16(a), x1 = lds_r16(a + 16384);
            k0 = mfma16(wfrag(Wk, ks), x0, k0); k1 = mfma16(wfrag(Wk, ks), x1, k1);
            v0 = mfma16(x0, wfrag(Wv, ks), v0); v1 = mfma16(x1, wfrag(Wv, ks), v1);
            q0 = mfma16(wfrag(Wq, ks), x0, q0); q1 = mfma16(wfrag(Wq, ks), x1, q1);
        }
        lptr kc = smem + P_OUT + c16 * 272 + (16 * w + 4 * g) * 2;
        lds_w8(kc + KQV_K, (u32x2){pk2(k0[0], k0[1]), pk2(k0[2], k0[3])});
        lds_w8(kc + KQV_K + 16 * 272, (u32x2){pk2(k1[0], k1[1]), pk2(k1[2], k1[3])});
        lds_w8(kc + KQV_Q, (u32x2){pk2(q0[0], q0[1]), pk2(q0[2], q0[3])});
        lds_w8(kc + KQV_Q + 16 * 272, (u32x2){pk2(q1[0], q1[1]), pk2(q1[2], q1[3])});
        const int e = 16 * w + c16;
        lds_w16(smem + P_OUT + KQV_V + e * 64 + ((g ^ ((4 - ((e >> 2) & 3)) & 3)) * 16), (u32x4){pk2(v0[0], v0[1]), pk2(v0[2], v0[3]), pk2(v1[0], v1[1]), pk2(v1[2], v1[3])});
        bar();
#pragma unroll
        for (int n = 0; n < 4; ++n)
            if (n < 3 || tid < (KQV_SZ - 24576) / 16) st16_dev(rk, n * 8192 + tid * 16, (unsigned)(s * 32 + h) * KQV_SZ, lds_r16(smem + P_OUT + n * 8192 + tid * 16));
    }
    vm0(); bar();
    if (tid == 0) stw_dev(p.ctrl + (L_PXP + h) * 64, NSEG);
    TSTAMP(3);
}

DI void seg_gemm(const u32x8 (&W)[8], lptr tile, int lane, f32x4& a0, f32x4& a1) {
    a0 = (f32x4){0, 0, 0, 0}; a1 = a0;
#pragma unroll
    for (int ks = 0; ks < 16; ++ks) {
        lptr a = afrag_ptr(tile, ks, lane);
        a0 = mfma16(wfrag(W, ks), lds_r16(a), a0);
        a1 = mfma16(wfrag(W, ks), lds_r16(a + 16384), a1);
    }
}
DI void put_f32_tile(lptr t, int w, int lane, f32x4 a0, f32x4 a1) {
    const int c16 = lane & 15, ch = 4 * w + (lane >> 4);
    lds_w16(t + c16 * 512 + (ch ^ c16) * 16, __builtin_bit_cast(u32x4, a0));
    lds_w16(t + (16 + c16) * 512 + (ch ^ c16) * 16, __builtin_bit_cast(u32x4, a1));
}
DI u32x4 get_f32_chunk(lptr t, int row, int ch) { return lds_r16(t + row * 512 + (ch ^ (row & 15)) * 16); }

constexpr int F_A = 0;
constexpr int F_T = 32768;
constexpr int F_W = 49152;
#define FO_SLEEP 8
#define MIX_SLEEP 16
DI void fo_role(const Params& p, int j, lptr smem, int tid) {
    const int lane = tid & 63, w = rfl(tid >> 6);
    const unsigned lbase = lds_addr(smem);
    const int cg = j & 3, kg = j >> 2;
    u32x8 W[8];
    load_slice<false>(W, p.Wo + (size_t)kg * 512 * 512 + cg * 128, 1024, smem, 65536, 0, w, lane);
    const rsrc_t rf = mkrs(p.big + O_FP, 64u * 32u * 16384u);
    const rsrc_t rox = mkrs(p.big + O_OX, 2048u * 8192u);
    TSTAMP(1);
    unsigned hx_known = 0;
    for (int s = 0; s < NSEG; ++s) {
        if (hx_known <= (unsigned)s) {
            if (w == 0) { const unsigned v = wait_words<FO_SLEEP>(p.ctrl, L_HXP + 8 * kg, 8, s + 1, lane); if (lane == 0) *(LAS unsigned*)(smem + F_W) = v; }
            bar();
            hx_known = rfl(*(LAS unsigned*)(smem + F_W));
            if (!hx_known) return;
        }
#pragma unroll
        for (int n = 0; n < 4; ++n) {
            const unsigned row = 4 * w + n, c = (lane & ~15) | ((lane ^ row) & 15);
            dmab16_dev(rox, (((row >> 4) * 32 + (c >> 4)) * 4 + ((c >> 2) & 3)) * 1024 + (row & 15) * 64 + (c & 3) * 16, (unsigned)(s * 64 + kg * 4) * 4096u, lbase + F_A + row * 1024);
        }
        vm0(); bar();
        if (s > 0 && tid == 0) stw_dev(p.ctrl + (L_FOP + j) * 64, s);
        f32x4 a0, a1;
        seg_gemm(W, smem + F_A, lane, a0, a1);
        put_f32_tile(smem + F_T, w, lane, a0, a1);
        bar();
        const unsigned dst = ((unsigned)(s * 8 + kg) * 4 + cg) * 16384u;
#pragma unroll
        for (int n = 0; n < 2; ++n) { const int ci = n * 512 + tid; st16_dev(rf, ci * 16, dst, get_f32_chunk(smem + F_T, ci >> 5, ci & 31)); }
        if (s == 0) TSTAMP(2);
    }
    vm0(); bar();
    if (tid == 0) stw_dev(p.ctrl + (L_FOP + j) * 64, NSEG);
    TSTAMP(3);
}

constexpr float SCL_B = 0.25f * 1.4426950408889634f;
constexpr int B_X = 0;
constexpr int B_T = 65536;
constexpr int B_G = 81920;
constexpr int B_K = 0, B_V = 65536, B_C = 131072;
template <bool SAFE> DI void b_attn_loop(lptr smem, int lane, u32x4 q0, u32x4 q1, f32x16& o0, f32x16& o1) {
    const int e = lane & 31;
    lptr kp = smem + B_K + lane * 16;
    lptr vp = e < 16 ? smem + B_V + (lane >> 5) * 256 + e * 16 : smem + B_C + (e == 16 ? 0 : 16);
    const int vstep = e < 16 ? 1024 : 0, vs = e < 16 ? 512 : 0;
    f32x16 z;
#pragma unroll
    for (int r = 0; r < 16; ++r) z[r] = 0;
    o0 = z; o1 = z;
    float m0 = -__builtin_inff(), m1 = m0;
#pragma unroll 2
    for (int kt = 0; kt < 64; ++kt) {
        const u32x4 kf = lds_r16(kp + kt * 1024);
        const u32x4 va = lds_r16(vp), vb = lds_r16(vp + vs);
        vp += vstep;
        f32x16 s0 = mfma32(kf, q0, z), s1 = mfma32(kf, q1, z);
        if (!SAFE) {
            float t0 = s0[0], t1 = s1[0];
#pragma unroll
            for (int r = 1; r < 16; ++r) { t0 = fmaxf(t0, s0[r]); t1 = fmaxf(t1, s1[r]); }
            t0 = fmaxf(max32(t0), m0); t1 = fmaxf(max32(t1), m1);
            const float c0 = ex2(m0 - t0), c1 = ex2(m1 - t1);
            m0 = t0; m1 = t1;
#pragma unroll
            for (int r = 0; r < 16; ++r) { s0[r] -= t0; s1[r] -= t1; o0[r] *= c0; o1[r] *= c1; }
        }
        u32x4 pa, pb, pc, pd;
#pragma unroll
        for (int r = 0; r < 4; ++r) {
            pa[r] = pk2(ex2(s0[2 * r]), ex2(s0[2 * r + 1])); pb[r] = pk2(ex2(s0[8 + 2 * r]), ex2(s0[9 + 2 * r]));
            pc[r] = pk2(ex2(s1[2 * r]), ex2(s1[2 * r + 1])); pd[r] = pk2(ex2(s1[8 + 2 * r]), ex2(s1[9 + 2 * r]));
        }
        o0 = mfma32(va, pa, o0); o0 = mfma32(vb, pb, o0);
        o1 = mfma32(va, pc, o1); o1 = mfma32(vb, pd, o1);
    }
}
DI float sq8(u32x4 v) {
    float s = 0;
#pragma unroll
    for (int r = 0; r < 4; ++r) { const float a = bf_lo(v[r]), b = bf_hi(v[r]); s = fmaf(a, a, s); s = fmaf(b, b, s); }
    return s;
}
DI void b_role(const Params& p, int i, lptr smem, int tid) {
    const int lane = tid & 63, w = rfl(tid >> 6), c16 = lane & 15, g = lane >> 4;
    const unsigned lbase = lds_addr(smem);
    u32x8 W[8];
    TSTAMP(0);
    if (i < 96) {
        const int cg = i % 12, rr = i / 12, kind = cg >> 2, head = 8 * (cg & 3) + w;
        load_slice<false>(W, p.wqkv + cg * 128, 3072, smem, 65536, 0, w, lane);
        const rsrc_t ro = mkrs(p.big + O_QB + (size_t)kind * 32 * 65536, 32u * 65536u);
        stage_rows<false>((const char*)p.x + (size_t)rr * 8 * 32768, 1024, lbase + B_X, w, lane);
        for (int n = 0; n < 8; ++n) {
            vm0(); bar();
            if (n + 1 < 8) stage_rows<false>((const char*)p.x + (size_t)(rr * 8 + n + 1) * 32768, 1024, lbase + B_X + ((n + 1) & 1) * 32768, w, lane);
            lptr tile = smem + B_X + (n & 1) * 32768;
            const unsigned tb = head * 65536u + (rr * 8 + n) * 1024u;
            if (kind < 2) {
                f32x4 a0, a1;
                seg_gemm(W, tile, lane, a0, a1);
                if (kind == 0) { a0 *= SCL_B; a1 *= SCL_B; }
                const unsigned o = tb + (g >> 1) * 512 + c16 * 16 + (g & 1) * 8;
                st8_dev(ro, o, (u32x2){pk2(a0[0], a0[1]), pk2(a0[2], a0[3])});
                st8_dev(ro, o + 256, (u32x2){pk2(a1[0], a1[1]), pk2(a1[2], a1[3])});
            } else {
                f32x4 a0 = {0, 0, 0, 0}, a1 = a0;
#pragma unroll
                for (int ks = 0; ks < 16; ++ks) {
                    lptr a = afrag_ptr(tile, ks, lane);
                    a0 = mfma16(lds_r16(a), wfrag(W, ks), a0);
                    a1 = mfma16(lds_r16(a + 16384), wfrag(W, ks), a1);
                }
                const unsigned o = tb + (g & 1) * 256 + c16 * 16 + (g >> 1) * 8;
                st8_dev(ro, o, (u32x2){pk2(a0[0], a0[1]), pk2(a0[2], a0[3])});
                st8_dev(ro, o + 512, (u32x2){pk2(a1[0], a1[1]), pk2(a1[2], a1[3])});
            }
        }
        vm0(); bar();
        if (tid == 0) addw_dev(p.ctrl + (L_BQC + cg) * 64, 1u);
    }
    TSTAMP(1);
    {
        const int h = i >> 2, qq = i & 3;
        if (w == 0) {
            const unsigned* cw = p.ctrl + (L_BQC + (h >> 3) + 4 * (lane < 3 ? lane : 0)) * 64;
            unsigned ok = 1;
            for (unsigned spins = 0;;) { if (__all(ldw_dev(cw) >= 8u)) break; if (spin_fail(spins, p.ctrl)) { ok = 0; break; } __builtin_amdgcn_s_sleep(8); }
            if (lane == 0) *(LAS unsigned*)(smem + B_C + 64) = ok;
        }
        bar();
        if (!rfl(*(LAS unsigned*)(smem + B_C + 64))) return;
        const char* kb = p.big + O_KB + (size_t)h * 65536, *vb = p.big + O_VB + (size_t)h * 65536;
#pragma unroll
        for (int n = 0; n < 8; ++n) {
            dma16_dev(kb + (w * 8 + n) * 1024 + lane * 16, lbase + B_K + (w * 8 + n) * 1024);
            dma16_dev(vb + (w * 8 + n) * 1024 + lane * 16, lbase + B_V + (w * 8 + n) * 1024);
        }
        const rsrc_t rq = mkrs(p.big + O_QB + (size_t)h * 65536, 65536u);
        const u32x4 q0 = ld16_dev(rq, (qq * 16 + 2 * w) * 1024 + lane * 16), q1 = ld16_dev(rq, (qq * 16 + 2 * w + 1) * 1024 + lane * 16);
        if (tid < 8) *(LAS unsigned*)(smem + B_C + tid * 4) = tid < 4 ? 0x3F803F80u : 0u;
        vm0(); bar();
        float kn = 0;
#pragma unroll
        for (int n = 0; n < 8; ++n) { const float t = sq8(lds_r16(smem + B_K + (w * 8 + n) * 1024 + lane * 16)); kn = fmaxf(kn, t + __shfl_xor(t, 32)); }
#pragma unroll
        for (int d = 1; d < 32; d <<= 1) kn = fmaxf(kn, __shfl_xor(kn, d));
        if (lane == 0) *(LAS float*)(smem + B_C + 128 + w * 4) = kn;
        bar();
#pragma unroll
        for (int n = 0; n < 8; ++n) kn = fmaxf(kn, *(LAS const float*)(smem + B_C + 128 + n * 4));
        float qn; { const float t0 = sq8(q0), t1 = sq8(q1); qn = fmaxf(t0 + __shfl_xor(t0, 32), t1 + __shfl_xor(t1, 32)); }
#pragma unroll
        for (int d = 1; d < 32; d <<= 1) qn = fmaxf(qn, __shfl_xor(qn, d));
        const bool safe = rfl(__float_as_uint(qn * kn)) <= __float_as_uint(60.0f * 60.0f);
        f32x16 o0, o1;
        TSTAMP(2);
        if (safe) b_attn_loop<true>(smem, lane, q0, q1, o0, o1); else b_attn_loop<false>(smem, lane, q0, q1, o0, o1);
        const rsrc_t ro = mkrs(p.big + O_OB + (size_t)h * 65536, 65536u);
#pragma unroll
        for (int t = 0; t < 2; ++t) {
            const f32x16& o = t ? o1 : o0;
            auto lr = __builtin_amdgcn_permlane32_swap(__float_as_uint(o[8]), __float_as_uint(o[8]), false, false);
            const float rl = 1.0f / __uint_as_float(lr[0]);
            const unsigned a = pk2(o[0] * rl, o[1] * rl), b = pk2(o[2] * rl, o[3] * rl), c = pk2(o[4] * rl, o[5] * rl), d = pk2(o[6] * rl, o[7] * rl);
            auto x = __builtin_amdgcn_permlane32_swap(a, c, false, false);
            auto y = __builtin_amdgcn_permlane32_swap(b, d, false, false);
            st16_dev(ro, (qq * 512 + w * 64 + t * 32 + (lane & 31)) * 32 + (lane >> 5) * 16, (u32x4){x[0], y[0], x[1], y[1]});
        }
        vm0(); bar();
        if (tid == 0) addw_dev(p.ctrl + (L_BAC + qq) * 64, 1u);
    }
    TSTAMP(3);
    {
        const int cg = i & 3, rr = i >> 2;
        load_slice<false>(W, p.wo + cg * 128, 1024, smem, 65536, 0, w, lane);
        {
            const u32x4 gw = *(const GAS u32x4*)(p.gw + lane * 8);
#pragma unroll
            for (int n = 0; n < 8; ++n) {
                const u32x4 xv = *(const GAS u32x4*)(p.x + (size_t)(64 * rr + 8 * w + n) * 512 + lane * 8);
                float z = 0;
#pragma unroll
                for (int r = 0; r < 4; ++r) { z = fmaf(bf_lo(xv[r]), bf_lo(gw[r]), z); z = fmaf(bf_hi(xv[r]), bf_hi(gw[r]), z); }
#pragma unroll
                for (int d = 1; d < 64; d <<= 1) z += __shfl_xor(z, d);
                if (lane == 0) *(LAS float*)(smem + B_G + (8 * w + n) * 4) = 1.0f / (1.0f + __expf(-z));
            }
        }
        if (w == 0) {
            unsigned ok = 1;
            for (unsigned spins = 0;;) { if (rfl(ldw_dev(p.ctrl + (L_BAC + (rr >> 3)) * 64)) >= 32u) break; if (spin_fail(spins, p.ctrl)) { ok = 0; break; } __builtin_amdgcn_s_sleep(8); }
            if (lane == 0) *(LAS unsigned*)(smem + B_G + 512) = ok;
        }
        bar();
        if (!rfl(*(LAS unsigned*)(smem + B_G + 512))) return;
        const rsrc_t rb = mkrs(p.big + O_BP, 2048u * 2048u + 8192u);
        const char* ob = p.big + O_OB;
        for (int n = 0; n < 2; ++n) {
            const int row0 = 64 * rr + 32 * n;
#pragma unroll
            for (int k = 0; k < 4; ++k) {
                const unsigned row = 4 * w + k, c = (lane & ~15) | ((lane ^ row) & 15);
                dma16_dev(ob + (size_t)(c >> 1) * 65536 + (row0 + row) * 32 + (c & 1) * 16, lbase + B_X + row * 1024);
            }
            vm0(); bar();
            f32x4 a0, a1;
            seg_gemm(W, smem + B_X, lane, a0, a1);
            const float g0 = 1.0f - *(LAS const float*)(smem + B_G + (32 * n + c16) * 4), g1 = 1.0f - *(LAS const float*)(smem + B_G + (32 * n + 16 + c16) * 4);
            put_f32_tile(smem + B_T, w, lane, a0 * g0, a1 * g1);
            bar();
#pragma unroll
            for (int k = 0; k < 2; ++k) { const int ci = k * 512 + tid; st16_dev(rb, (row0 + (ci >> 5)) * 2048 + cg * 512 + (ci & 31) * 16, get_f32_chunk(smem + B_T, ci >> 5, ci & 31)); }
            bar();
        }
        if (cg == 0 && tid < 64) st4_dev(rb, 2048u * 2048u + (64 * rr + tid) * 4, *(LAS const unsigned*)(smem + B_G + tid * 4));
        vm0(); bar();
        if (tid == 0) addw_dev(p.ctrl + (L_BOC + rr) * 64, 1u);
    }
    TSTAMP(4);
    {
        const rsrc_t rf = mkrs(p.big + O_FP, 64u * 32u * 16384u);
        const rsrc_t rb = mkrs(p.big + O_BP, 2048u * 2048u + 8192u);
        lptr red = smem + B_X;
        unsigned fo_known = 0;
        for (int n = 0; n < 16; ++n) {
            const int row = 128 * n + i, sg = row >> 5, j = row & 31;
            if (w == 0) {
                unsigned ok = 1;
                if (fo_known <= (unsigned)sg) { fo_known = wait_words<MIX_SLEEP>(p.ctrl, L_FOP, 32, sg + 1, lane); ok = fo_known; }
                if (ok) ok = wait_words(p.ctrl, L_BOC + (row >> 6), 1, 4, lane);
                if (lane == 0) *(LAS unsigned*)(smem + B_G + 512) = ok;
            }
            bar();
            if (!rfl(*(LAS unsigned*)(smem + B_G + 512))) return;
            const unsigned src = ((unsigned)(sg * 8 + (tid >> 7)) * 4 + ((tid >> 5) & 3)) * 16384u + j * 512 + (tid & 31) * 16;
            const u32x4 v0 = ld16_dev(rf, src), v1 = ld16_dev(rf, src + 4 * 4 * 16384u);
            f32x2 bp = {0, 0}; float gt = 0;
            if (tid < 256) {
                bp = __builtin_bit_cast(f32x2, __builtin_amdgcn_raw_buffer_load_b64(rb, row * 2048 + tid * 8, 0, 16));
                gt = __uint_as_float(__builtin_amdgcn_raw_buffer_load_b32(rb, 2048u * 2048u + row * 4, 0, 16));
            }
            lds_w16(red + (tid >> 7) * 2048 + (tid & 127) * 16, __builtin_bit_cast(u32x4, __builtin_bit_cast(f32x4, v0) + __builtin_bit_cast(f32x4, v1)));
            bar();
            if (tid < 256) {
                f32x2 t = {0, 0};
#pragma unroll
                for (int k = 0; k < 4; ++k) t += *(LAS const f32x2*)(red + k * 2048 + tid * 8);
                *(GAS unsigned*)(p.out + (size_t)row * 512 + tid * 2) = pk2(fmaf(gt, t.x, bp.x), fmaf(gt, t.y, bp.y));
            }
        }
    }
    TSTAMP(5);
}

#define LAYOUT 1
__global__ void __launch_bounds__(512, 2) gm_kernel(Params p) {
    extern __shared__ __attribute__((aligned(16))) char smem_raw[];
    lptr smem = (lptr)smem_raw;
    const int b = blockIdx.x, tid = threadIdx.x;
    const int idx = (b >> 3) * 4 + (b & 3);
    const int role = (b & 4) ? (idx < 32 ? 1 : idx < 64 ? 2 : 3) : (idx < 64 ? 0 : 3);
    const int ri = role == 2 ? idx - 32 : role == 3 ? ((b & 4) ? idx : idx - 64) : idx;
    if (role == 3) b_role(p, ri, smem, tid);
    else if (role == 0) head_role(p, ri, smem, tid);
    else if (role == 1) px_role(p, ri, smem, tid);
    else fo_role(p, ri, smem, tid);
}

extern "C" void kernel_launch(void* const* d_in, const int* in_sizes, int n_in, void* d_out, int out_size, void* d_ws, size_t ws_size, hipStream_t stream) {
    Params p;
    p.x = (const u16*)d_in[0]; p.mem0 = (const u16*)d_in[1]; p.Wq = (const u16*)d_in[2]; p.Wk = (const u16*)d_in[3]; p.Wv = (const u16*)d_in[4]; p.Wo = (const u16*)d_in[5];
    p.wqkv = (const u16*)d_in[8]; p.wo = (const u16*)d_in[9]; p.gw = (const u16*)d_in[10];
    p.out = (u16*)d_out;
    p.ctrl = (unsigned*)d_ws;
    const size_t a = ((size_t)d_ws + CTRL_BYTES + (2u << 20) - 1) & ~(size_t)((2u << 20) - 1);
    p.exch = (char*)a;
    p.big = (char*)a + (2u << 20);
    (void)hipFuncSetAttribute((const void*)gm_kernel, hipFuncAttributeMaxDynamicSharedMemorySize, LDS_BYTES);
    (void)hipMemsetAsync(d_ws, 0, CTRL_BYTES, stream);
    hipLaunchKernelGGL(gm_kernel, dim3(256), dim3(512), LDS_BYTES, stream, p);
}
```
